```python
import torch

def _softmax(x, axis=-1):
    return torch.softmax(x, dim=axis)
def _gelu(x):
    return torch.nn.functional.gelu(x, approximate="tanh")
def _silu(x):
    return torch.nn.functional.silu(x)
def _sigmoid(x):
    return torch.sigmoid(x)
def _layernorm(x, axis=-1, eps=1e-5):
    m = x.mean(dim=axis, keepdim=True)
    v = x.var(dim=axis, keepdim=True, unbiased=False)
    return (x - m) / torch.sqrt(v + eps)
def _scatter_add(tgt, idx, src, axis=0):
    return tgt.clone().index_add_(axis, idx, src)
def _segment_sum(src, seg, n):
    out = torch.zeros((n,) + src.shape[1:], dtype=src.dtype)
    return out.index_add_(0, seg, src)
def _pad(x, pw):
    flat = tuple(v for pair in reversed(pw) for v in pair)
    return torch.nn.functional.pad(x, flat)

kernel_name = 'recurrent_scan__linear_attn_block__mixer_s_mlp__prompt'

def setup_inputs():

    _w = (lambda a: (lambda t: t.to(torch.bfloat16) if t.is_floating_point() else t)(torch.as_tensor(a)))
    def _A_setup():
        B, S, D = 1, 2048, 640
        return {
            "x": _w((lambda *s: torch.randn(*s, dtype=torch.bfloat16) * 0.05)(B, S, D)),
            "wqkv": _w((lambda *s: torch.randn(*s, dtype=torch.bfloat16) * 0.05)(D, 3 * D)),
            "wo": _w((lambda *s: torch.randn(*s, dtype=torch.bfloat16) * 0.05)(D, D)),
        }
    _A_in = _A_setup()

    def _B_setup():
        B, S, D, F = 1, 2048, 640, 1536
        return {
            "x":  _w((lambda *s: torch.randn(*s, dtype=torch.bfloat16) * 0.05)(B, S, D)),
            "w1": _w((lambda *s: torch.randn(*s, dtype=torch.bfloat16) * 3.2)(D, D) * (D ** -0.5)),
            "w2": _w((lambda *s: torch.randn(*s, dtype=torch.bfloat16) * 3.2)(D, D) * (D ** -0.5)),
            "w3": _w((lambda *s: torch.randn(*s, dtype=torch.bfloat16) * 3.2)(D, F) * (D ** -0.5)),
            "w4": _w((lambda *s: torch.randn(*s, dtype=torch.bfloat16) * 1.6)(F, D) * (F ** -0.5)),
        }
    _B_in = _B_setup()

    return {
        "A_x": _A_in["x"],
        "A_wqkv": _A_in["wqkv"],
        "A_wo": _A_in["wo"],
        "B_x": _B_in["x"],
        "B_w1": _B_in["w1"],
        "B_w2": _B_in["w2"],
        "B_w3": _B_in["w3"],
        "B_w4": _B_in["w4"],
    }

def reference(A_x, A_wqkv, A_wo, B_x, B_w1, B_w2, B_w3, B_w4):
    def _A_ref(x, wqkv, wo):
        B, S, D = x.shape
        H = 32; Dh = D // H
        qkv = (lambda x, *d: x.permute(*d))((x @ wqkv).reshape(B, S, 3, H, Dh), 2, 0, 3, 1, 4)
        q, k, v = qkv[0], qkv[1], qkv[2]
        phi = lambda t: torch.where(t > 0, t + 1, torch.exp(t)) + 1e-6
        pq, pk = phi(q), phi(k)
        kv = torch.einsum("bhsd,bhse->bhde", pk, v)
        z = torch.einsum("bhsd->bhd", pk)
        num = torch.einsum("bhqd,bhde->bhqe", pq, kv)
        den = torch.einsum("bhqd,bhd->bhq", pq, z)[..., None] + 1e-6
        o = num / den
        return (lambda x, *d: x.permute(*d))(o, 0, 2, 1, 3).reshape(B, S, D) @ wo

    def _B_ref(x, w1, w2, w3, w4):
        B, S, D = x.shape
        h = _gelu(_layernorm(x, axis=-1) @ w1)
        c = D // 4
        g0 = _pad(h[:, :, 0*c:1*c], ((0, 0), (1, 0), (0, 0)))[:, :S, :]
        g1 = _pad(h[:, :, 1*c:2*c], ((0, 0), (0, 1), (0, 0)))[:, 1:, :]
        g2 = _pad(h[:, :, 2*c:3*c], ((0, 0), (2, 0), (0, 0)))[:, :S, :]
        g3 = _pad(h[:, :, 3*c:   ], ((0, 0), (0, 2), (0, 0)))[:, 2:, :]
        hs = torch.concatenate([g0, g1, g2, g3], axis=-1) @ w2
        x1 = x + hs
        h2 = _gelu(_layernorm(x1, axis=-1) @ w3) @ w4
        return x1 + h2

    x = A_x
    xn = _layernorm(x, axis=-1)
    s = _A_ref(xn, A_wqkv, A_wo)
    g = _silu(_B_ref(xn, B_w1, B_w2, B_w3, B_w4))
    return x + s * g
```

```cpp
#include <hip/hip_runtime.h>
#include <hip/hip_bf16.h>
#include <stdint.h>

typedef unsigned short bf16_t;
__device__ __forceinline__ float bf2f(bf16_t v){ return __uint_as_float(((unsigned)v)<<16); }
__device__ __forceinline__ bf16_t f2bf(float f){ unsigned u=__float_as_uint(f); u = (u + 0x7FFF + ((u>>16)&1))>>16; return (bf16_t)u; }
__device__ __forceinline__ float rb(float f){ return bf2f(f2bf(f)); }
constexpr int S=2048, D=640, F=1536, H=32, DH=20;

__global__ void k_ln(const bf16_t* x, float* y, int isbf){}

__global__ void ln_bf(const bf16_t* x, float* y){
  int r = blockIdx.x; __shared__ float red[256];
  float s=0; for(int c=threadIdx.x;c<D;c+=256) s+=bf2f(x[r*D+c]);
  red[threadIdx.x]=s; __syncthreads();
  for(int o=128;o>0;o>>=1){ if(threadIdx.x<o) red[threadIdx.x]+=red[threadIdx.x+o]; __syncthreads(); }
  float m=red[0]/D; __syncthreads();
  s=0; for(int c=threadIdx.x;c<D;c+=256){ float d=bf2f(x[r*D+c])-m; s+=d*d; }
  red[threadIdx.x]=s; __syncthreads();
  for(int o=128;o>0;o>>=1){ if(threadIdx.x<o) red[threadIdx.x]+=red[threadIdx.x+o]; __syncthreads(); }
  float v=red[0]/D; float rs=1.0f/sqrtf(v+1e-5f);
  for(int c=threadIdx.x;c<D;c+=256) y[r*D+c]=rb((bf2f(x[r*D+c])-m)*rs);
}
__global__ void ln_f(const float* x, float* y){
  int r = blockIdx.x; __shared__ float red[256];
  float s=0; for(int c=threadIdx.x;c<D;c+=256) s+=x[r*D+c];
  red[threadIdx.x]=s; __syncthreads();
  for(int o=128;o>0;o>>=1){ if(threadIdx.x<o) red[threadIdx.x]+=red[threadIdx.x+o]; __syncthreads(); }
  float m=red[0]/D; __syncthreads();
  s=0; for(int c=threadIdx.x;c<D;c+=256){ float d=x[r*D+c]-m; s+=d*d; }
  red[threadIdx.x]=s; __syncthreads();
  for(int o=128;o>0;o>>=1){ if(threadIdx.x<o) red[threadIdx.x]+=red[threadIdx.x+o]; __syncthreads(); }
  float v=red[0]/D; float rs=1.0f/sqrtf(v+1e-5f);
  for(int c=threadIdx.x;c<D;c+=256) y[r*D+c]=rb((x[r*D+c]-m)*rs);
}
__device__ __forceinline__ float gelu(float x){ float u=0.7978845608028654f*(x+0.044715f*x*x*x); return 0.5f*x*(1.0f+tanhf(u)); }
__global__ void gemm(const float* A, const bf16_t* W, float* C, int M, int N, int K, int mode){
  int n = blockIdx.x*64 + (threadIdx.x&63); int m = blockIdx.y*4 + (threadIdx.x>>6);
  if(n>=N) return;
  float acc=0; for(int k=0;k<K;k++) acc += A[m*K+k]*bf2f(W[k*N+n]);
  if(mode==1) acc=gelu(rb(acc));
  C[m*N+n]=rb(acc);
}
__global__ void phik(float* qkv){
  int i = blockIdx.x*256+threadIdx.x; int r=i/(2*D), c=i%(2*D);
  float t=qkv[r*3*D+c]; t = (t>0? t+1.0f : __expf(t)) + 1e-6f; qkv[r*3*D+c]=rb(t);
}
__global__ void kvk(const float* qkv, float* kv, float* z){
  int h=blockIdx.x; int t=threadIdx.x;
  if(t<400){ int d=t/20,e=t%20; float a=0; for(int s=0;s<S;s++) a+=qkv[s*3*D+D+h*DH+d]*qkv[s*3*D+2*D+h*DH+e]; kv[h*400+t]=rb(a);}
  else if(t<420){ int d=t-400; float a=0; for(int s=0;s<S;s++) a+=qkv[s*3*D+D+h*DH+d]; z[h*DH+d]=rb(a);}
}
__global__ void ok(const float* qkv, const float* kv, const float* z, float* o){
  int i=blockIdx.x*256+threadIdx.x; int r=i/D,c=i%D; int h=c/DH,e=c%DH;
  float n=0,d=0; for(int k=0;k<DH;k++){ float q=qkv[r*3*D+h*DH+k]; n+=q*kv[h*400+k*20+e]; d+=q*z[h*DH+k]; }
  o[i]=rb(rb(n)/(rb(d)+1e-6f));
}
__global__ void shiftk(const float* h, float* o){
  int i=blockIdx.x*256+threadIdx.x; int r=i/D,c=i%D; int g=c/160; int sh = g==0?-1: g==1?1: g==2?-2:2;
  int rr=r+sh; o[i]=(rr>=0&&rr<S)? h[rr*D+c]:0.f;
}
__global__ void addk(const float* a,const float* b,float* o){ int i=blockIdx.x*256+threadIdx.x; o[i]=rb(a[i]+b[i]); }
__global__ void fin(const bf16_t* x,const float* s,const float* x1,const float* h2,bf16_t* out){
  int i=blockIdx.x*256+threadIdx.x; float b=rb(x1[i]+h2[i]); float g=rb(b/(1.0f+__expf(-b))); out[i]=f2bf(bf2f(x[i])+rb(s[i]*g));
}
extern "C" void kernel_launch(void* const* d_in, const int* in_sizes, int n_in, void* d_out, int out_size, void* d_ws, size_t ws_size, hipStream_t stream){
  const bf16_t* x=(const bf16_t*)d_in[0]; const bf16_t* wqkv=(const bf16_t*)d_in[1]; const bf16_t* wo=(const bf16_t*)d_in[2];
  const bf16_t* w1=(const bf16_t*)d_in[4]; const bf16_t* w2=(const bf16_t*)d_in[5]; const bf16_t* w3=(const bf16_t*)d_in[6]; const bf16_t* w4=(const bf16_t*)d_in[7];
  float* ws=(float*)d_ws; size_t o=0;
  float* xn=ws+o; o+=S*D; float* qkv=ws+o; o+=S*3*D; float* kv=ws+o; o+=H*400; float* z=ws+o; o+=D; float* ob=ws+o; o+=S*D;
  float* sb=ws+o; o+=S*D; float* l2=ws+o; o+=S*D; float* h=ws+o; o+=S*D; float* hs=ws+o; o+=S*D; float* t=ws+o; o+=S*D; float* x1=ws+o; o+=S*D;
  float* h3=ws+o; o+=S*F; float* h2=ws+o; o+=S*D;
  ln_bf<<<S,256,0,stream>>>(x,xn);
  gemm<<<dim3(3*D/64,S/4),256,0,stream>>>(xn,wqkv,qkv,S,3*D,D,0);
  phik<<<S*2*D/256,256,0,stream>>>(qkv);
  kvk<<<H,448,0,stream>>>(qkv,kv,z);
  ok<<<S*D/256,256,0,stream>>>(qkv,kv,z,ob);
  gemm<<<dim3(D/64,S/4),256,0,stream>>>(ob,wo,sb,S,D,D,0);
  ln_f<<<S,256,0,stream>>>(xn,l2);
  gemm<<<dim3(D/64,S/4),256,0,stream>>>(l2,w1,h,S,D,D,1);
  shiftk<<<S*D/256,256,0,stream>>>(h,hs);
  gemm<<<dim3(D/64,S/4),256,0,stream>>>(hs,w2,t,S,D,D,0);
  addk<<<S*D/256,256,0,stream>>>(xn,t,x1);
  ln_f<<<S,256,0,stream>>>(x1,l2);
  gemm<<<dim3(F/64,S/4),256,0,stream>>>(l2,w3,h3,S,F,D,1);
  gemm<<<dim3(D/64,S/4),256,0,stream>>>(h3,w4,h2,S,D,F,0);
  fin<<<S*D/256,256,0,stream>>>(x,sb,x1,h2,(bf16_t*)d_out);
}
```

```cpp
#include <hip/hip_runtime.h>
#include <stdint.h>

typedef __attribute__((ext_vector_type(8))) __bf16 bf16x8;
typedef __attribute__((ext_vector_type(2))) __bf16 bf16x2;
typedef __attribute__((ext_vector_type(4))) float f32x4;
typedef __attribute__((ext_vector_type(2))) float f32x2;
typedef __attribute__((ext_vector_type(4))) short v4i16;
typedef __attribute__((ext_vector_type(8))) short v8i16;
typedef __attribute__((ext_vector_type(4))) unsigned u32x4;
typedef __attribute__((ext_vector_type(2))) unsigned u32x2;
#define LAS __attribute__((address_space(3)))
#define DI __device__ __forceinline__

constexpr int S_ = 2048, D_ = 640, F_ = 1536;
constexpr size_t OFF_PK = 0;
constexpr size_t OFF_V = OFF_PK + 64 * 40 * 1024;
constexpr size_t OFF_H = OFF_V + 64 * 40 * 1024;
constexpr size_t OFF_X1 = OFF_H + 128 * 20 * 1024;
constexpr size_t OFF_HP = OFF_X1 + 128 * 20 * 1024;
constexpr size_t OFF_ZP = OFF_HP + 128 * 68 * 1024;
constexpr size_t OFF_ST = OFF_ZP + 16 * 640 * 4;
constexpr size_t OFF_KVP = OFF_ST + 2048 * 8;
constexpr size_t OFF_ME = OFF_KVP + 4 * 32 * 400 * 4;
constexpr float EPS = 1e-5f;

DI void dma16(const void* sbase, unsigned voff, unsigned ldsdst) {
  unsigned keep;
  asm volatile("s_mov_b32 %0, m0\n\ts_mov_b32 m0, %3\n\ts_nop 0\n\tglobal_load_lds_dwordx4 %1, %2\n\ts_mov_b32 m0, %0"
               : "=&s"(keep) : "v"(voff), "s"(sbase), "s"(ldsdst) : "memory");
}
#define BAR() asm volatile("s_waitcnt lgkmcnt(0)\n\ts_barrier" ::: "memory")
template <int N> DI void waitvm() { asm volatile("s_waitcnt vmcnt(%0)" ::"n"(N) : "memory"); }
DI float dot2(unsigned a, unsigned b, float c) {
  return __builtin_amdgcn_fdot2_f32_bf16(__builtin_bit_cast(bf16x2, a), __builtin_bit_cast(bf16x2, b), c, false);
}
DI unsigned pk2(float lo, float hi) { f32x2 v = {lo, hi}; bf16x2 b = __builtin_convertvector(v, bf16x2); return __builtin_bit_cast(unsigned, b); }
DI float bflo(unsigned u) { return __uint_as_float(u << 16); }
DI float bfhi(unsigned u) { return __uint_as_float(u & 0xffff0000u); }
DI float sigm(float z) { return __builtin_amdgcn_rcpf(1.0f + __expf(-z)); }
DI float gelu(float x) { float u = 1.5957691216057308f * (x + 0.044715f * x * x * x); return x * sigm(u); }
DI float phi(float t) { return (t > 0.f ? t + 1.0f : __expf(t)) + 1e-6f; }
DI float shx(float v, int m) { return __shfl_xor(v, m, 64); }
DI bf16x8 lds128(LAS const char* p) { return *(LAS const bf16x8*)p; }
DI v4i16 ldstr(LAS const char* p) { return __builtin_amdgcn_ds_read_tr16_b64_v4i16((LAS v4i16*)p); }
DI bf16x8 cat(v4i16 a, v4i16 b) { v8i16 r = __builtin_shufflevector(a, b, 0, 1, 2, 3, 4, 5, 6, 7); return __builtin_bit_cast(bf16x8, r); }

template <int MB_, int NB_, int NB0_, int KS_, int D_R, bool XROW_>
struct Cfg {
  static constexpr int MB = MB_, NB = NB_, NB0 = NB0_, KS = KS_, D = D_R;
  static constexpr bool XROW = XROW_;
  static constexpr int MBW = MB / 2, RB = NB * 32, TOKB = MB * KS * 1024, WB = KS * 32 * RB, STAGE = TOKB + WB;
  static constexpr int PT = TOKB / 4096, PW = WB / 4096, P = PT + PW;
  static constexpr int RING = D * STAGE;
  static constexpr int NBW = NB0;
};
typedef Cfg<8, 8, 4, 2, 4, true> CfgA1;
typedef Cfg<8, 8, 4, 2, 4, false> CfgA;
typedef Cfg<4, 10, 5, 2, 4, true> CfgQ;
typedef Cfg<4, 5, 3, 4, 4, false> CfgC;
constexpr int LDS_AUX = 147456;
constexpr int LDS_TOTAL = LDS_AUX + 4096;

template <int RB> DI int wswz(int rho) { return RB == 256 ? 2 * (rho & 7) : (RB == 320 ? 2 * ((rho >> 2) & 1) : 0); }

template <class C, bool TSTAT, bool CSUM>
DI void loader(int w, int lane, unsigned lds0, LAS char* lds, const char* tokbase, int tokstride, const unsigned (&tokv)[C::PT],
               const char* wb1, const char* wb2, int S1, int wstride, const unsigned (&wv)[C::PW], int NS) {
  auto issue = [&](int s) {
    const char* tb = tokbase + (size_t)s * tokstride;
    const char* wb = s < S1 ? wb1 + (size_t)s * wstride : wb2 + (size_t)(s - S1) * wstride;
    unsigned dst = lds0 + (s % C::D) * C::STAGE + w * 1024;
#pragma unroll
    for (int i = 0; i < C::PT; i++) dma16(tb, tokv[i], dst + i * 4096);
#pragma unroll
    for (int i = 0; i < C::PW; i++) dma16(wb, wv[i], dst + C::TOKB + i * 4096);
  };
  for (int s = 0; s < C::D; s++) issue(s);
  constexpr int NT = C::XROW ? C::MB / 2 : C::MB / 4;
  constexpr int NCB = (C::NB + 3) / 4;
  float s1[NT], s2[NT], cs[NCB];
#pragma unroll
  for (int i = 0; i < NT; i++) s1[i] = s2[i] = 0.f;
#pragma unroll
  for (int i = 0; i < NCB; i++) cs[i] = 0.f;
  const int g = lane >> 4, q = (lane >> 2) & 3, p = lane & 3;
  for (int j = 0; j < NS; j++) {
    int rem = NS - 1 - j;
    if (j == 0) waitvm<C::P*(C::D - 1)>();
    else if (rem >= C::D - 2) waitvm<C::P*(C::D - 2)>();
    else if (rem == 1) waitvm<C::P>();
    else waitvm<0>();
    BAR();
    if (j >= 1 && j - 1 + C::D < NS) issue(j - 1 + C::D);
    LAS const char* slot = lds + (j % C::D) * C::STAGE;
    if (TSTAT) {
      if (C::XROW) {
#pragma unroll
        for (int i = 0; i < NT; i++) {
          u32x4 v = *(LAS const u32x4*)(slot + (w * NT + i) * 1024 + lane * 16);
#pragma unroll
          for (int e = 0; e < 4; e++) { s1[i] = dot2(v[e], 0x3f803f80u, s1[i]); s2[i] = dot2(v[e], v[e], s2[i]); }
        }
      } else {
#pragma unroll
        for (int i = 0; i < NT; i++)
#pragma unroll
          for (int ks = 0; ks < C::KS; ks++) {
            u32x4 v = *(LAS const u32x4*)(slot + ((w * NT + i) * C::KS + ks) * 1024 + lane * 16);
#pragma unroll
            for (int e = 0; e < 4; e++) { s1[i] = dot2(v[e], 0x3f803f80u, s1[i]); s2[i] = dot2(v[e], v[e], s2[i]); }
          }
      }
    }
    if (CSUM) {
#pragma unroll
      for (int i = 0; i < NCB; i++) {
        int nb = w + 4 * i;
        if (nb < C::NB) {
#pragma unroll
          for (int ks = 0; ks < C::KS; ks++)
#pragma unroll
            for (int t = 0; t < 2; t++) {
              int rho = 32 * ks + 16 * t + 4 * g + q;
              u32x2 v = __builtin_bit_cast(u32x2, ldstr(slot + C::TOKB + rho * C::RB + (((2 * nb + (p >> 1)) ^ wswz<C::RB>(rho)) << 4) + (p & 1) * 8));
              cs[i] = dot2(v[0], 0x3f803f80u, cs[i]); cs[i] = dot2(v[1], 0x3f803f80u, cs[i]);
            }
        }
      }
    }
  }
  LAS float* aux = (LAS float*)(lds + LDS_AUX);
  if (TSTAT) {
#pragma unroll
    for (int i = 0; i < NT; i++) {
      float a = s1[i], b = s2[i];
      int row; bool wr;
      if (C::XROW) { a += shx(a, 1); a += shx(a, 2); a += shx(a, 4); b += shx(b, 1); b += shx(b, 2); b += shx(b, 4); row = (w * NT + i) * 8 + (lane >> 3); wr = (lane & 7) == 0; }
      else { a += shx(a, 16); a += shx(a, 32); b += shx(b, 16); b += shx(b, 32); row = (w * NT + i) * 16 + (lane & 15); wr = lane < 16; }
      float m = a * (1.0f / D_), var = fmaxf(b * (1.0f / D_) - m * m, 0.f);
      if (wr) { aux[2 * row] = m; aux[2 * row + 1] = var; }
    }
  }
  if (CSUM) {
#pragma unroll
    for (int i = 0; i < NCB; i++) {
      float a = cs[i]; a += shx(a, 16); a += shx(a, 32);
      int nb = w + 4 * i;
      if (nb < C::NB && lane < 16) aux[256 + nb * 16 + lane] = a;
    }
  }
  BAR();
}

template <class C> DI void make_wv(unsigned (&wv)[C::PW], int w, int lane, int pitch) {
#pragma unroll
  for (int i = 0; i < C::PW; i++) {
    int sidx = (i * 4 + w) * 64 + lane;
    constexpr int SL = C::RB / 16;
    int rho = sidx / SL, c = sidx % SL;
    int k = C::XROW ? ((rho & ~31) | (((rho >> 2) & 3) << 3) | (((rho >> 4) & 1) << 2) | (rho & 3)) : rho;
    wv[i] = k * pitch + ((c ^ wswz<C::RB>(rho)) << 4);
  }
}
template <class C> DI void make_tokv(unsigned (&tv)[C::PT], int w, int lane, int kstot) {
#pragma unroll
  for (int i = 0; i < C::PT; i++) {
    int qd = i * 4 + w;
    if (C::XROW) { int row = qd * 8 + (lane >> 3); tv[i] = row * 1280 + (((lane & 7) ^ ((row >> 1) & 7)) << 4); }
    else { int mb = qd / C::KS, ks = qd % C::KS; tv[i] = (mb * kstot + ks) * 1024 + lane * 16; }
  }
}

template <class C, int NBW> struct Frags { bf16x8 t[C::MBW]; bf16x8 w[NBW]; };
template <class C> struct CAddr { int tok[2]; int wa[4]; };
template <class C> DI void make_caddr(CAddr<C>& a, int wm, int wn, int lane) {
  const int g = lane >> 4, q = (lane >> 2) & 3, p = lane & 3, t = lane & 15;
  if (C::XROW) {
    a.tok[0] = (wm * C::MBW * 16 + t) * 128 + ((g ^ ((t >> 1) & 7)) << 4);
    a.tok[1] = (wm * C::MBW * 16 + t) * 128 + (((4 + g) ^ ((t >> 1) & 7)) << 4);
  } else { a.tok[0] = wm * C::MBW * C::KS * 1024 + lane * 16; a.tok[1] = 0; }
  int r0 = 4 * g + q, base = C::TOKB + r0 * C::RB + (p >> 1) * 16 + (p & 1) * 8;
  if (C::RB == 256) {
#pragma unroll
    for (int j = 0; j < 4; j++) a.wa[j] = base + (((wn * 4 + j) ^ (r0 & 7)) << 5);
  } else if (C::RB == 320) {
    int b = g & 1, n0 = wn * C::NB0;
    a.wa[0] = base + ((n0 ^ b) << 5);
    a.wa[1] = base + (((n0 + 1) ^ b) << 5) - 32;
    a.wa[2] = a.wa[3] = 0;
  } else { a.wa[0] = base + wn * C::NB0 * 32; a.wa[1] = a.wa[2] = a.wa[3] = 0; }
}
template <class C, int NBW>
DI void load_frags(Frags<C, NBW>& f, LAS const char* slot, const CAddr<C>& a, int ks) {
#pragma unroll
  for (int i = 0; i < C::MBW; i++)
    f.t[i] = C::XROW ? lds128(slot + a.tok[ks & 1] + i * 2048) : lds128(slot + a.tok[0] + (i * C::KS + ks) * 1024);
#pragma unroll
  for (int j = 0; j < NBW; j++) {
    LAS const char* pw = C::RB == 256 ? slot + a.wa[j] : (C::RB == 320 ? slot + a.wa[j & 1] + j * 32 : slot + a.wa[0] + j * 32);
    f.w[j] = cat(ldstr(pw + ks * 32 * C::RB), ldstr(pw + (ks * 32 + 16) * C::RB));
  }
}
template <class C, int NBW, bool SWAP>
DI void mma(f32x4 (&acc)[C::MBW][NBW], const Frags<C, NBW>& f) {
#pragma unroll
  for (int j = 0; j < NBW; j++)
#pragma unroll
    for (int i = 0; i < C::MBW; i++)
      acc[i][j] = SWAP ? __builtin_amdgcn_mfma_f32_16x16x32_bf16(f.w[j], f.t[i], acc[i][j], 0, 0, 0)
                       : __builtin_amdgcn_mfma_f32_16x16x32_bf16(f.t[i], f.w[j], acc[i][j], 0, 0, 0);
}
template <class C, int NBW, bool SWAP>
DI void consume(f32x4 (&acc)[C::MBW][NBW], Frags<C, NBW>& fa, LAS const char* lds, const CAddr<C>& a, int s0, int s1, int NSTOT) {
  for (int s = s0; s < s1; s++) {
    LAS const char* slot = lds + (s % C::D) * C::STAGE;
    Frags<C, NBW> fb;
#pragma unroll
    for (int ks = 0; ks < C::KS; ks += 2) {
      load_frags<C, NBW>(fb, slot, a, ks + 1);
      mma<C, NBW, SWAP>(acc, fa);
      if (ks + 2 < C::KS) load_frags<C, NBW>(fa, slot, a, ks + 2);
      else if (s + 1 < NSTOT) { BAR(); load_frags<C, NBW>(fa, lds + ((s + 1) % C::D) * C::STAGE, a, 0); }
      mma<C, NBW, SWAP>(acc, fb);
    }
  }
}
template <int A, int B> DI void zero(f32x4 (&acc)[A][B]) {
#pragma unroll
  for (int i = 0; i < A; i++)
#pragma unroll
    for (int j = 0; j < B; j++) acc[i][j] = f32x4{0.f, 0.f, 0.f, 0.f};
}

extern __shared__ __attribute__((aligned(16))) char smem[];

__global__ __launch_bounds__(512) void phase1(const char* __restrict__ x, const char* __restrict__ wqkv, const char* __restrict__ w1, char* __restrict__ ws) {
  typedef CfgA1 C;
  const int b = blockIdx.x, xcd = b & 7, idx = b >> 3;
  const int rt = (xcd >> 1) * 4 + (idx & 3), ct = (xcd & 1) * 8 + (idx >> 2);
  if (ct >= 15) return;
  const int wid = __builtin_amdgcn_readfirstlane(threadIdx.x >> 6), lane = threadIdx.x & 63;
  LAS char* lds = (LAS char*)smem;
  if (wid >= 4) {
    const int w = wid - 4;
    unsigned tv[C::PT], wv[C::PW];
    const char* wb; int pitch;
    if (ct < 10) { wb = wqkv + (640 + ct * 128) * 2; pitch = 3840; } else { wb = w1 + (ct - 10) * 256; pitch = 1280; }
    make_tokv<C>(tv, w, lane, 0); make_wv<C>(wv, w, lane, pitch);
    loader<C, true, true>(w, lane, (unsigned)(size_t)lds, lds, x + (size_t)rt * 128 * 1280, 128, tv, wb, wb, 1 << 20, 64 * pitch, wv, 10);
    return;
  }
  const int wm = wid >> 1, wn = wid & 1, g = lane >> 4, tk = lane & 15;
  CAddr<C> a; make_caddr<C>(a, wm, wn, lane);
  f32x4 acc[4][4]; zero(acc);
  Frags<C, 4> fa;
  BAR(); load_frags<C, 4>(fa, lds, a, 0);
  LAS const float* aux = (LAS const float*)(lds + LDS_AUX);
  if (ct < 10) {
    consume<C, 4, false>(acc, fa, lds, a, 0, 10, 10);
    BAR();
    float zs[4] = {0.f, 0.f, 0.f, 0.f};
    char* dst = ws + (ct < 5 ? OFF_PK : OFF_V);
    const int cb0 = (ct < 5 ? ct : ct - 5) * 8 + wn * 4;
#pragma unroll
    for (int ip = 0; ip < 2; ip++) {
      float r[8], rm[8];
#pragma unroll
      for (int h = 0; h < 2; h++)
#pragma unroll
        for (int e = 0; e < 4; e++) {
          int row = wm * 64 + (2 * ip + h) * 16 + 4 * g + e;
          float m = aux[2 * row], var = aux[2 * row + 1];
          float rs = __builtin_amdgcn_rsqf(var + EPS);
          r[h * 4 + e] = rs; rm[h * 4 + e] = rs * m;
        }
#pragma unroll
      for (int j = 0; j < 4; j++) {
        float cs = aux[256 + wn * 64 + j * 16 + tk];
        float v[8];
#pragma unroll
        for (int h = 0; h < 2; h++)
#pragma unroll
          for (int e = 0; e < 4; e++) {
            float t = r[h * 4 + e] * acc[2 * ip + h][j][e] - rm[h * 4 + e] * cs;
            v[h * 4 + e] = ct < 5 ? phi(t) : t;
          }
        u32x4 o = {pk2(v[0], v[1]), pk2(v[2], v[3]), pk2(v[4], v[5]), pk2(v[6], v[7])};
        if (ct < 5) {
#pragma unroll
          for (int e = 0; e < 4; e++) zs[j] += bflo(o[e]) + bfhi(o[e]);
        }
        *(u32x4*)(dst + ((size_t)((rt * 4 + wm * 2 + ip) * 40 + cb0 + j) * 64 + lane) * 16) = o;
      }
    }
    if (ct < 5) {
      LAS float* zl = (LAS float*)(lds + LDS_AUX + 2048);
#pragma unroll
      for (int j = 0; j < 4; j++) { zs[j] += shx(zs[j], 16); zs[j] += shx(zs[j], 32); }
      if (wm == 1 && lane < 16) {
#pragma unroll
        for (int j = 0; j < 4; j++) zl[wn * 64 + j * 16 + lane] = zs[j];
      }
      BAR();
      if (wm == 0 && lane < 16) {
        float* zp = (float*)(ws + OFF_ZP) + rt * 640 + ct * 128 + wn * 64;
#pragma unroll
        for (int j = 0; j < 4; j++) zp[j * 16 + lane] = zs[j] + zl[wn * 64 + j * 16 + lane];
      }
      if (ct == 0 && wm == 1 && wn == 0) {
        f32x2* st = (f32x2*)(ws + OFF_ST) + rt * 128;
        for (int r = lane; r < 128; r += 64) st[r] = f32x2{aux[2 * r], __builtin_amdgcn_rsqf(aux[2 * r + 1] + EPS)};
      }
    }
  } else {
    consume<C, 4, true>(acc, fa, lds, a, 0, 10, 10);
    BAR();
    const int c0 = (ct - 10) * 128 + wn * 64;
#pragma unroll
    for (int i = 0; i < 4; i++) {
      int row = wm * 64 + i * 16 + tk;
      float m = aux[2 * row], var = aux[2 * row + 1];
      float rs = __builtin_amdgcn_rsqf(var + EPS);
      float r2 = __builtin_amdgcn_rsqf(var * rs * rs + EPS);
      float rr = rs * r2, rm = rr * m;
#pragma unroll
      for (int jp = 0; jp < 2; jp++) {
        float v[8];
#pragma unroll
        for (int h = 0; h < 2; h++) {
          f32x4 cs = *(LAS const f32x4*)(aux + 256 + wn * 64 + (2 * jp + h) * 16 + 4 * g);
#pragma unroll
          for (int e = 0; e < 4; e++) v[h * 4 + e] = gelu(rr * acc[i][2 * jp + h][e] - rm * cs[e]);
        }
        int col = c0 + jp * 32, grp = col / 160;
        int dl = grp == 0 ? 1 : (grp == 1 ? -1 : (grp == 2 ? 2 : -2));
        int tsrc = rt * 128 + row, td = tsrc + dl;
        bool ok = td >= 0 && td < S_;
        td &= S_ - 1;
        u32x4 o = {pk2(v[0], v[1]), pk2(v[2], v[3]), pk2(v[4], v[5]), pk2(v[6], v[7])};
        if (!ok) o = u32x4{0, 0, 0, 0};
        *(u32x4*)(ws + OFF_H + ((size_t)((td >> 4) * 20 + (col >> 5)) * 64 + g * 16 + (td & 15)) * 16) = o;
      }
    }
  }
}

__global__ __launch_bounds__(512) void phase2(const char* __restrict__ x, const char* __restrict__ wqkv, const char* __restrict__ w2, char* __restrict__ ws) {
  const int b = blockIdx.x;
  const int wid = __builtin_amdgcn_readfirstlane(threadIdx.x >> 6), lane = threadIdx.x & 63;
  LAS char* lds = (LAS char*)smem;
  const int g = lane >> 4, tk = lane & 15;
  LAS const float* aux = (LAS const float*)(lds + LDS_AUX);
  if (b < 128) {
    typedef CfgQ C;
    const int rt = b >> 2, ct = b & 3;
    if (wid >= 4) {
      const int w = wid - 4;
      unsigned tv[C::PT], wv[C::PW];
      make_tokv<C>(tv, w, lane, 0); make_wv<C>(wv, w, lane, 3840);
      const char* wb = wqkv + ct * 320;
      loader<C, true, true>(w, lane, (unsigned)(size_t)lds, lds, x + (size_t)rt * 64 * 1280, 128, tv, wb, wb, 1 << 20, 64 * 3840, wv, 10);
      return;
    }
    const int wm = wid >> 1, wn = wid & 1;
    {
      int t = threadIdx.x;
      if (t < 160) {
        const float* zp = (const float*)(ws + OFF_ZP) + ct * 160 + t;
        float z = 0.f;
#pragma unroll
        for (int r = 0; r < 16; r++) z += zp[r * 640];
        ((LAS float*)(lds + LDS_AUX))[512 + t] = z;
      }
    }
    CAddr<C> a; make_caddr<C>(a, wm, wn, lane);
    f32x4 acc[2][5]; zero(acc);
    Frags<C, 5> fa;
    BAR(); load_frags<C, 5>(fa, lds, a, 0);
    consume<C, 5, true>(acc, fa, lds, a, 0, 10, 10);
    BAR();
#pragma unroll
    for (int i = 0; i < 2; i++) {
      int row = wm * 32 + i * 16 + tk;
      float m = aux[2 * row], var = aux[2 * row + 1];
      float rs = __builtin_amdgcn_rsqf(var + EPS), rm = rs * m;
      float pq[5][4], part[5];
#pragma unroll
      for (int j = 0; j < 5; j++) {
        f32x4 cs = *(LAS const f32x4*)(aux + 256 + wn * 80 + j * 16 + 4 * g);
        f32x4 z = *(LAS const f32x4*)(aux + 512 + wn * 80 + j * 16 + 4 * g);
        part[j] = 0.f;
#pragma unroll
        for (int e = 0; e < 4; e++) { pq[j][e] = phi(rs * acc[i][j][e] - rm * cs[e]); part[j] += pq[j][e] * z[e]; }
      }
      float dh[4];
      dh[0] = part[0] + (g == 0 ? part[1] : 0.f);
      dh[1] = (g >= 1 ? part[1] : 0.f) + (g <= 1 ? part[2] : 0.f);
      dh[2] = (g >= 2 ? part[2] : 0.f) + (g <= 2 ? part[3] : 0.f);
      dh[3] = (g == 3 ? part[3] : 0.f) + part[4];
#pragma unroll
      for (int h = 0; h < 4; h++) { dh[h] += shx(dh[h], 16); dh[h] += shx(dh[h], 32); dh[h] = __builtin_amdgcn_rcpf(dh[h] + 1e-6f); }
      float sc[5] = {dh[0], g == 0 ? dh[0] : dh[1], g <= 1 ? dh[1] : dh[2], g <= 2 ? dh[2] : dh[3], dh[3]};
      char* dst = ws + OFF_HP + ((size_t)(rt * 4 + wm * 2 + i) * 68 + 48 + ct * 5) * 1024 + lane * 16;
      *(u32x4*)(dst + (3 * wn) * 1024) = u32x4{pk2(pq[0][0] * sc[0], pq[0][1] * sc[0]), pk2(pq[0][2] * sc[0], pq[0][3] * sc[0]), pk2(pq[1][0] * sc[1], pq[1][1] * sc[1]), pk2(pq[1][2] * sc[1], pq[1][3] * sc[1])};
      *(u32x4*)(dst + (3 * wn + 1) * 1024) = u32x4{pk2(pq[2][0] * sc[2], pq[2][1] * sc[2]), pk2(pq[2][2] * sc[2], pq[2][3] * sc[2]), pk2(pq[3][0] * sc[3], pq[3][1] * sc[3]), pk2(pq[3][2] * sc[3], pq[3][3] * sc[3])};
      *(u32x2*)(dst + 2 * 1024 + wn * 8) = u32x2{pk2(pq[4][0] * sc[4], pq[4][1] * sc[4]), pk2(pq[4][2] * sc[4], pq[4][3] * sc[4])};
    }
  } else if (b < 208) {
    typedef CfgA C;
    const int bb = b - 128, rt = bb / 5, ct = bb % 5;
    if (wid >= 4) {
      const int w = wid - 4;
      unsigned tv[C::PT], wv[C::PW];
      make_tokv<C>(tv, w, lane, 20); make_wv<C>(wv, w, lane, 1280);
      const char* wb = w2 + ct * 256;
      loader<C, false, false>(w, lane, (unsigned)(size_t)lds, lds, ws + OFF_H + (size_t)rt * 8 * 20 * 1024, 2048, tv, wb, wb, 1 << 20, 64 * 1280, wv, 10);
      return;
    }
    const int wm = wid >> 1, wn = wid & 1;
    u32x2 xv[4][4]; f32x2 st[4];
#pragma unroll
    for (int i = 0; i < 4; i++) {
      int row = rt * 128 + wm * 64 + i * 16 + tk;
      st[i] = ((const f32x2*)(ws + OFF_ST))[row];
#pragma unroll
      for (int j = 0; j < 4; j++) xv[i][j] = *(const u32x2*)(x + (size_t)row * 1280 + (ct * 128 + wn * 64 + j * 16 + 4 * g) * 2);
    }
    CAddr<C> a; make_caddr<C>(a, wm, wn, lane);
    f32x4 acc[4][4]; zero(acc);
    Frags<C, 4> fa;
    BAR(); load_frags<C, 4>(fa, lds, a, 0);
    consume<C, 4, true>(acc, fa, lds, a, 0, 10, 10);
    BAR();
#pragma unroll
    for (int i = 0; i < 4; i++) {
      float m = st[i][0], rs = st[i][1];
#pragma unroll
      for (int jp = 0; jp < 2; jp++) {
        u32x4 o;
#pragma unroll
        for (int h = 0; h < 2; h++) {
          u32x2 xx = xv[i][2 * jp + h]; f32x4 c = acc[i][2 * jp + h];
          o[2 * h] = pk2((bflo(xx[0]) - m) * rs + c[0], (bfhi(xx[0]) - m) * rs + c[1]);
          o[2 * h + 1] = pk2((bflo(xx[1]) - m) * rs + c[2], (bfhi(xx[1]) - m) * rs + c[3]);
        }
        *(u32x4*)(ws + OFF_X1 + ((size_t)(rt * 8 + wm * 4 + i) * 20 + ct * 4 + wn * 2 + jp) * 1024 + lane * 16) = o;
      }
    }
  } else if (b < 240) {
    const int bb = b - 208, G = bb & 7, R = bb >> 3;
    f32x4 acc[13];
#pragma unroll
    for (int i = 0; i < 13; i++) acc[i] = f32x4{0.f, 0.f, 0.f, 0.f};
    bf16x8 pk[2][5], vv[2][5];
#pragma unroll
    for (int u = 0; u < 2; u++)
#pragma unroll
      for (int j = 0; j < 5; j++) {
        size_t o = ((size_t)(R * 16 + wid * 2 + u) * 40 + G * 5 + j) * 1024 + lane * 16;
        pk[u][j] = *(const bf16x8*)(ws + OFF_PK + o); vv[u][j] = *(const bf16x8*)(ws + OFF_V + o);
      }
#pragma unroll
    for (int u = 0; u < 2; u++)
#pragma unroll
      for (int db = 0; db < 5; db++)
#pragma unroll
        for (int eb = db - 1; eb <= db + 1; eb++)
          if (eb >= 0 && eb < 5) acc[2 * db + eb] = __builtin_amdgcn_mfma_f32_16x16x32_bf16(pk[u][db], vv[u][eb], acc[2 * db + eb], 0, 0, 0);
#pragma unroll
    for (int i = 0; i < 13; i++) *(LAS f32x4*)(lds + (wid * 13 + i) * 1024 + lane * 16) = acc[i];
    __syncthreads();
    for (int o = threadIdx.x; o < 1600; o += 512) {
      int hh = o / 400, d = (o % 400) / 20, e = o % 20;
      int da = 20 * hh + d, ea = 20 * hh + e;
      int fi = (2 * (da >> 4) + (ea >> 4)) * 256 + (((da & 15) >> 2) * 16 + (ea & 15)) * 4 + (da & 3);
      float s = 0.f;
#pragma unroll
      for (int w = 0; w < 8; w++) s += ((LAS const float*)lds)[w * 13 * 256 + fi];
      ((float*)(ws + OFF_KVP))[(R * 32 + G * 4 + hh) * 400 + d * 20 + e] = s;
    }
  }
}

__global__ __launch_bounds__(512) void phase3(const char* __restrict__ wo, const char* __restrict__ w3, char* __restrict__ ws) {
  const int b = blockIdx.x;
  const int wid = __builtin_amdgcn_readfirstlane(threadIdx.x >> 6), lane = threadIdx.x & 63;
  LAS char* lds = (LAS char*)smem;
  if (b < 192) {
    typedef CfgA C;
    const int rt = b / 12, ct = b % 12;
    if (wid >= 4) {
      const int w = wid - 4;
      unsigned tv[C::PT], wv[C::PW];
      make_tokv<C>(tv, w, lane, 20); make_wv<C>(wv, w, lane, 3072);
      const char* wb = w3 + ct * 256;
      loader<C, true, true>(w, lane, (unsigned)(size_t)lds, lds, ws + OFF_X1 + (size_t)rt * 8 * 20 * 1024, 2048, tv, wb, wb, 1 << 20, 64 * 3072, wv, 10);
      return;
    }
    const int wm = wid >> 1, wn = wid & 1, g = lane >> 4, tk = lane & 15;
    LAS const float* aux = (LAS const float*)(lds + LDS_AUX);
    CAddr<C> a; make_caddr<C>(a, wm, wn, lane);
    f32x4 acc[4][4]; zero(acc);
    Frags<C, 4> fa;
    BAR(); load_frags<C, 4>(fa, lds, a, 0);
    consume<C, 4, true>(acc, fa, lds, a, 0, 10, 10);
    BAR();
#pragma unroll
    for (int i = 0; i < 4; i++) {
      int row = wm * 64 + i * 16 + tk;
      float m = aux[2 * row], var = aux[2 * row + 1];
      float rs = __builtin_amdgcn_rsqf(var + EPS), rm = rs * m;
#pragma unroll
      for (int jp = 0; jp < 2; jp++) {
        float v[8];
#pragma unroll
        for (int h = 0; h < 2; h++) {
          f32x4 cs = *(LAS const f32x4*)(aux + 256 + wn * 64 + (2 * jp + h) * 16 + 4 * g);
#pragma unroll
          for (int e = 0; e < 4; e++) v[h * 4 + e] = gelu(rs * acc[i][2 * jp + h][e] - rm * cs[e]);
        }
        u32x4 o = {pk2(v[0], v[1]), pk2(v[2], v[3]), pk2(v[4], v[5]), pk2(v[6], v[7])};
        *(u32x4*)(ws + OFF_HP + ((size_t)(rt * 8 + wm * 4 + i) * 68 + ct * 4 + wn * 2 + jp) * 1024 + lane * 16) = o;
      }
    }
  } else {
    const int bb = b - 192, h = bb >> 1, half = bb & 1, t = threadIdx.x;
    LAS float* kv = (LAS float*)lds;
    if (t < 400) {
      const float* p = (const float*)(ws + OFF_KVP) + h * 400 + t;
      kv[t] = p[0] + p[32 * 400] + p[2 * 32 * 400] + p[3 * 32 * 400];
    }
    const int cp = t % 160, dg = t / 160;
    unsigned wv[20];
    if (dg < 3) {
#pragma unroll
      for (int e = 0; e < 20; e++) wv[e] = *(const unsigned*)(wo + (size_t)(20 * h + e) * 1280 + (half * 320 + cp * 2) * 2);
    }
    __syncthreads();
    if (dg < 3) {
      int d0 = dg * 7, d1 = dg == 2 ? 20 : d0 + 7;
      for (int d = d0; d < d1; d++) {
        float a0 = 0.f, a1 = 0.f;
#pragma unroll
        for (int e = 0; e < 20; e++) { float k = kv[d * 20 + e]; a0 += k * bflo(wv[e]); a1 += k * bfhi(wv[e]); }
        int c = 20 * h + d, beta = c >> 4, G = beta / 10, j = beta % 10, wn = j / 5, jj = j % 5;
        int kap = jj == 4 ? 5 * G + 2 : 5 * G + 3 * wn + (jj >> 1), tt = jj == 4 ? wn : (jj & 1);
        int row = 32 * kap + 16 * tt + (c & 15);
        *(unsigned*)(ws + OFF_ME + (size_t)row * 1280 + (half * 320 + cp * 2) * 2) = pk2(a0, a1);
      }
    }
  }
}

template <int NBW>
DI void p4_consumer(const char* __restrict__ x, char* __restrict__ out, const char* __restrict__ ws, LAS char* lds, int rt, int ct, int wm, int wn, int lane) {
  typedef CfgC C;
  const int g = lane >> 4, tk = lane & 15;
  u32x2 xv[2][NBW], x1v[2][NBW];
#pragma unroll
  for (int i = 0; i < 2; i++) {
    int row = rt * 64 + wm * 32 + i * 16 + tk;
#pragma unroll
    for (int j = 0; j < NBW; j++) {
      int blk = ct * 5 + wn * 3 + j;
      xv[i][j] = *(const u32x2*)(x + (size_t)row * 1280 + (blk * 16 + 4 * g) * 2);
      x1v[i][j] = *(const u32x2*)(ws + OFF_X1 + ((size_t)(row >> 4) * 20 + (blk >> 1)) * 1024 + lane * 16 + (blk & 1) * 8);
    }
  }
  CAddr<C> a; make_caddr<C>(a, wm, wn, lane);
  f32x4 h2[2][NBW], sa[2][NBW]; zero(h2); zero(sa);
  Frags<C, NBW> fa;
  BAR(); load_frags<C, NBW>(fa, lds, a, 0);
  consume<C, NBW, true>(h2, fa, lds, a, 0, 12, 17);
  consume<C, NBW, true>(sa, fa, lds, a, 12, 17, 17);
  BAR();
#pragma unroll
  for (int i = 0; i < 2; i++) {
    int row = rt * 64 + wm * 32 + i * 16 + tk;
#pragma unroll
    for (int j = 0; j < NBW; j++) {
      int blk = ct * 5 + wn * 3 + j;
      float xx[4] = {bflo(xv[i][j][0]), bfhi(xv[i][j][0]), bflo(xv[i][j][1]), bfhi(xv[i][j][1])};
      float x1[4] = {bflo(x1v[i][j][0]), bfhi(x1v[i][j][0]), bflo(x1v[i][j][1]), bfhi(x1v[i][j][1])};
      float o[4];
#pragma unroll
      for (int e = 0; e < 4; e++) { float bb = x1[e] + h2[i][j][e]; o[e] = xx[e] + sa[i][j][e] * (bb * sigm(bb)); }
      *(u32x2*)(out + (size_t)row * 1280 + (blk * 16 + 4 * g) * 2) = u32x2{pk2(o[0], o[1]), pk2(o[2], o[3])};
    }
  }
}
__global__ __launch_bounds__(512) void phase4(const char* __restrict__ x, const char* __restrict__ w4, char* __restrict__ ws, char* __restrict__ out) {
  typedef CfgC C;
  const int b = blockIdx.x, rt = b >> 3, ct = b & 7;
  const int wid = __builtin_amdgcn_readfirstlane(threadIdx.x >> 6), lane = threadIdx.x & 63;
  LAS char* lds = (LAS char*)smem;
  if (wid >= 4) {
    const int w = wid - 4;
    unsigned tv[C::PT], wv[C::PW];
    make_tokv<C>(tv, w, lane, 68); make_wv<C>(wv, w, lane, 1280);
    loader<C, false, false>(w, lane, (unsigned)(size_t)lds, lds, ws + OFF_HP + (size_t)rt * 4 * 68 * 1024, 4096, tv, w4 + ct * 160, ws + OFF_ME + ct * 160, 12, 128 * 1280, wv, 17);
    return;
  }
  const int wm = wid >> 1, wn = wid & 1;
  if (wn == 0) p4_consumer<3>(x, out, ws, lds, rt, ct, wm, wn, lane);
  else p4_consumer<2>(x, out, ws, lds, rt, ct, wm, wn, lane);
}

extern "C" void kernel_launch(void* const* d_in, const int* in_sizes, int n_in, void* d_out, int out_size, void* d_ws, size_t ws_size, hipStream_t stream) {
  const char* x = (const char*)d_in[0]; const char* wqkv = (const char*)d_in[1]; const char* wo = (const char*)d_in[2];
  const char* w1 = (const char*)d_in[4]; const char* w2 = (const char*)d_in[5]; const char* w3 = (const char*)d_in[6]; const char* w4 = (const char*)d_in[7];
  char* ws = (char*)d_ws;
  hipFuncSetAttribute((const void*)phase1, hipFuncAttributeMaxDynamicSharedMemorySize, LDS_TOTAL);
  hipFuncSetAttribute((const void*)phase2, hipFuncAttributeMaxDynamicSharedMemorySize, LDS_TOTAL);
  hipFuncSetAttribute((const void*)phase3, hipFuncAttributeMaxDynamicSharedMemorySize, LDS_TOTAL);
  hipFuncSetAttribute((const void*)phase4, hipFuncAttributeMaxDynamicSharedMemorySize, LDS_TOTAL);
  phase1<<<256, 512, LDS_TOTAL, stream>>>(x, wqkv, w1, ws);
  phase2<<<240, 512, LDS_TOTAL, stream>>>(x, wqkv, w2, ws);
  phase3<<<256, 512, LDS_TOTAL, stream>>>(wo, w3, ws);
  phase4<<<256, 512, LDS_TOTAL, stream>>>(x, w4, ws, (char*)d_out);
}
```

```cpp
#include <hip/hip_runtime.h>
#include <stdint.h>

typedef __attribute__((ext_vector_type(8))) __bf16 bf16x8;
typedef __attribute__((ext_vector_type(2))) __bf16 bf16x2;
typedef __attribute__((ext_vector_type(4))) float f32x4;
typedef __attribute__((ext_vector_type(2))) float f32x2;
typedef __attribute__((ext_vector_type(4))) short v4i16;
typedef __attribute__((ext_vector_type(8))) short v8i16;
typedef __attribute__((ext_vector_type(4))) unsigned u32x4;
typedef __attribute__((ext_vector_type(2))) unsigned u32x2;
#define LAS __attribute__((address_space(3)))
#define DI __device__ __forceinline__
#define SB() __builtin_amdgcn_sched_barrier(0)

constexpr int S_ = 2048, D_ = 640, F_ = 1536;
constexpr size_t OFF_PK = 0;
constexpr size_t OFF_V = OFF_PK + 64 * 40 * 1024;
constexpr size_t OFF_H = OFF_V + 64 * 40 * 1024;
constexpr size_t OFF_X1 = OFF_H + 128 * 20 * 1024;
constexpr size_t OFF_HP = OFF_X1 + 128 * 20 * 1024;
constexpr size_t OFF_WT = OFF_HP + 128 * 68 * 1024;
constexpr size_t OFF_ZP = OFF_WT + 68 * 40 * 1024;
constexpr size_t OFF_ST = OFF_ZP + 16 * 640 * 4;
constexpr size_t OFF_XS = OFF_ST + 2048 * 8;
constexpr size_t OFF_KVP = OFF_XS + 2048 * 80;
constexpr float EPS = 1e-5f;
#define STAMP(k, i)
#define STAMPEND(k)

DI void dma16(const void* sbase, unsigned voff, unsigned ldsdst) {
  unsigned keep;
  asm volatile("s_mov_b32 %0, m0\n\ts_mov_b32 m0, %3\n\ts_nop 0\n\tglobal_load_lds_dwordx4 %1, %2\n\ts_mov_b32 m0, %0"
               : "=&s"(keep) : "v"(voff), "s"(sbase), "s"(ldsdst) : "memory");
}
#define BAR() asm volatile("s_waitcnt lgkmcnt(0)\n\ts_barrier" ::: "memory")
template <int N> DI void waitvm() { asm volatile("s_waitcnt vmcnt(%0)" ::"n"(N) : "memory"); }
DI unsigned pk2(float lo, float hi) { f32x2 v = {lo, hi}; bf16x2 b = __builtin_convertvector(v, bf16x2); return __builtin_bit_cast(unsigned, b); }
DI unsigned pk2v(f32x2 v) { bf16x2 b = __builtin_convertvector(v, bf16x2); return __builtin_bit_cast(unsigned, b); }
DI float bflo(unsigned u) { return __uint_as_float(u << 16); }
DI float bfhi(unsigned u) { return __uint_as_float(u & 0xffff0000u); }
DI f32x2 unpk(unsigned u) { return f32x2{bflo(u), bfhi(u)}; }
DI float ex2(float x) { return __builtin_amdgcn_exp2f(x); }
DI float rcp(float x) { return __builtin_amdgcn_rcpf(x); }
DI f32x2 lo2(f32x4 v) { return f32x2{v[0], v[1]}; }
DI f32x2 hi2(f32x4 v) { return f32x2{v[2], v[3]}; }
DI f32x2 gelu2(f32x2 t) {
  const float c1 = -1.5957691216057308f * 1.4426950408889634f, c2 = c1 * 0.044715f;
  f32x2 u = t * t;
  f32x2 z = t * (u * c2 + c1);
  f32x2 d = f32x2{ex2(z[0]), ex2(z[1])} + 1.0f;
  return t * f32x2{rcp(d[0]), rcp(d[1])};
}
DI f32x2 phi2(f32x2 t) {
  f32x2 a = t + (1.0f + 1e-6f), z = t * 1.4426950408889634f;
  f32x2 e = f32x2{ex2(z[0]), ex2(z[1])} + 1e-6f;
  return f32x2{t[0] > 0.f ? a[0] : e[0], t[1] > 0.f ? a[1] : e[1]};
}
DI float shx(float v, int m) { return __shfl_xor(v, m, 64); }
DI bf16x8 lds128(LAS const char* p) { return *(LAS const bf16x8*)p; }
DI v4i16 ldstr(LAS const char* p) { return __builtin_amdgcn_ds_read_tr16_b64_v4i16((LAS v4i16*)p); }
DI bf16x8 cat(v4i16 a, v4i16 b) { v8i16 r = __builtin_shufflevector(a, b, 0, 1, 2, 3, 4, 5, 6, 7); return __builtin_bit_cast(bf16x8, r); }
DI f32x4 mfma(bf16x8 a, bf16x8 b, f32x4 c) { return __builtin_amdgcn_mfma_f32_16x16x32_bf16(a, b, c, 0, 0, 0); }

template <int MB_, int NB_, int NB0_, int KS_, int D_R, bool XROW_, bool FRAGW_>
struct Cfg {
  static constexpr int MB = MB_, NB = NB_, NB0 = NB0_, KS = KS_, D = D_R;
  static constexpr bool XROW = XROW_, FRAGW = FRAGW_;
  static constexpr int MBW = MB / 2, RB = NB * 32, TOKB = MB * KS * 1024, WB = KS * 32 * RB, STAGE = TOKB + WB;
  static constexpr int PT = TOKB / 4096, PW = WB / 4096, P = PT + PW;
};
typedef Cfg<8, 8, 4, 2, 4, true, false> CfgA1;
typedef Cfg<8, 8, 4, 2, 4, false, false> CfgA;
typedef Cfg<4, 10, 5, 2, 4, true, false> CfgQ;
typedef Cfg<4, 5, 3, 4, 4, false, true> CfgC;
constexpr int LDS_AUX = 147456;
constexpr int LDS_TOTAL = LDS_AUX + 4096;

template <int RB> DI unsigned wswz(unsigned rho) { return RB == 256 ? 2 * (rho & 7) : (RB == 320 ? 2 * ((rho >> 2) & 1) : 0); }

template <class C>
DI void loader(int w, unsigned lds0, const char* tokbase, int tokstride, const unsigned (&tokv)[C::PT],
               const char* wb, int wstride, const unsigned (&wv)[C::PW], int NS) {
  static_assert(C::D == 4, "ring depth");
  auto issue = [&](int s) {
    const char* tb = tokbase + (size_t)s * tokstride;
    const char* wp = wb + (size_t)s * wstride;
    unsigned dst = lds0 + (s & 3) * C::STAGE + w * 1024;
#pragma unroll
    for (int i = 0; i < C::PT; i++) dma16(tb, tokv[i], dst + i * 4096);
#pragma unroll
    for (int i = 0; i < C::PW; i++) dma16(wp, wv[i], dst + C::TOKB + i * 4096);
  };
  issue(0); issue(1);
  waitvm<C::P>(); BAR();
  issue(2); issue(3);
  waitvm<2 * C::P>(); BAR();
  issue(4);
#pragma unroll 1
  for (int j = 2; j < NS - 2; j++) {
    waitvm<2 * C::P>(); BAR();
    if (j + 3 < NS) issue(j + 3);
  }
  waitvm<C::P>(); BAR();
  waitvm<0>(); BAR();
}

template <class C> DI void make_wv(unsigned (&wv)[C::PW], unsigned w, unsigned lane, unsigned pitch) {
#pragma unroll
  for (int i = 0; i < C::PW; i++) {
    if (C::FRAGW) {
      unsigned pc = i * 4 + w, ks = pc / C::NB, nb = pc % C::NB;
      wv[i] = (ks * 40 + nb) * 1024 + lane * 16;
    } else {
      unsigned sidx = (i * 4 + w) * 64 + lane;
      constexpr unsigned SL = C::RB / 16;
      unsigned rho = sidx / SL, c = sidx % SL;
      unsigned k = C::XROW ? ((rho & ~31u) | (((rho >> 2) & 3) << 3) | (((rho >> 4) & 1) << 2) | (rho & 3)) : rho;
      wv[i] = k * pitch + ((c ^ wswz<C::RB>(rho)) << 4);
    }
  }
}
template <class C> DI void make_tokv(unsigned (&tv)[C::PT], unsigned w, unsigned lane, unsigned kstot) {
#pragma unroll
  for (int i = 0; i < C::PT; i++) {
    unsigned qd = i * 4 + w;
    if (C::XROW) { unsigned row = qd * 8 + (lane >> 3); tv[i] = row * 1280 + (((lane & 7) ^ ((row >> 1) & 7)) << 4); }
    else { unsigned mb = qd / C::KS, ks = qd % C::KS; tv[i] = (mb * kstot + ks) * 1024 + lane * 16; }
  }
}

template <class C, int NBW> struct Frags { bf16x8 t[C::MBW]; v4i16 wl[NBW], wh[NBW]; bf16x8 wf[NBW]; };
template <class C> struct CAddr { int tok[2]; int tokb[2]; int wa[4]; };
template <class C> DI void make_caddr(CAddr<C>& a, int wm, int wn, int lane, int pi = 0, int pj = 0) {
  const int g = lane >> 4, q = (lane >> 2) & 3, p = lane & 3, t = lane & 15;
  if (C::XROW) {
    a.tok[0] = (wm * C::MBW * 16 + t) * 128 + ((g ^ ((t >> 1) & 7)) << 4);
    a.tok[1] = (wm * C::MBW * 16 + t) * 128 + (((4 + g) ^ ((t >> 1) & 7)) << 4);
  } else { a.tok[0] = wm * C::MBW * C::KS * 1024 + lane * 16; a.tok[1] = 0; }
  {
    const int rot = pi * (C::XROW ? 2048 : C::KS * 1024);
    a.tokb[0] = a.tok[0] - rot; a.tokb[1] = a.tok[1] - rot; a.tok[0] += rot; a.tok[1] += rot;
  }
  int r0 = 4 * g + q, base = C::TOKB + r0 * C::RB + (p >> 1) * 16 + (p & 1) * 8;
  if (C::FRAGW) { a.wa[0] = C::TOKB + wn * C::NB0 * 1024 + lane * 16; a.wa[1] = a.wa[2] = a.wa[3] = 0; }
  else if (C::RB == 256) {
#pragma unroll
    for (int j = 0; j < 4; j++) a.wa[j] = base + (((wn * 4 + ((j + pj) & 3)) ^ (r0 & 7)) << 5);
  } else {
    int b = g & 1, n0 = wn * C::NB0;
    a.wa[0] = base + ((n0 ^ b) << 5);
    a.wa[1] = base + (((n0 + 1) ^ b) << 5) - 32;
    a.wa[2] = a.wa[3] = 0;
  }
}
template <class C, int NBW> struct RD { static constexpr int WR = C::FRAGW ? 1 : 2, NR = C::MBW + WR * NBW; };
template <class C, int NBW>
DI void read_one(Frags<C, NBW>& f, LAS const char* slot, const CAddr<C>& a, int ks, int r) {
  constexpr int WR = RD<C, NBW>::WR;
  int ti = -1, wj = -1, h = 0;
  if (r == 0) ti = 0;
  else if (r <= WR) { wj = 0; h = r - 1; }
  else if (r < WR + C::MBW) ti = r - WR;
  else { int q = r - C::MBW; wj = q / WR; h = q % WR; }
  if (ti >= 0) {
    const bool hi = ti >= C::MBW / 2;
    f.t[ti] = C::XROW ? lds128(slot + (hi ? a.tokb[ks & 1] : a.tok[ks & 1]) + ti * 2048)
                      : lds128(slot + (hi ? a.tokb[0] : a.tok[0]) + (ti * C::KS + ks) * 1024);
  } else if (C::FRAGW) {
    f.wf[wj] = lds128(slot + a.wa[0] + (ks * C::NB + wj) * 1024);
  } else {
    LAS const char* pw = C::RB == 256 ? slot + a.wa[wj] : slot + a.wa[wj & 1] + wj * 32;
    if (h == 0) f.wl[wj] = ldstr(pw + ks * 32 * C::RB);
    else f.wh[wj] = ldstr(pw + (ks * 32 + 16) * C::RB);
  }
}
template <class C, int NBW> DI bf16x8 wfrag(const Frags<C, NBW>& f, int j) { return C::FRAGW ? f.wf[j] : cat(f.wl[j], f.wh[j]); }
template <class C, int NBW>
DI void load_all(Frags<C, NBW>& f, LAS const char* slot, const CAddr<C>& a, int ks) {
#pragma unroll
  for (int r = 0; r < RD<C, NBW>::NR; r++) read_one<C, NBW>(f, slot, a, ks, r);
}
template <int NCS_, int CSM_, int NTS_> struct Extra {
  static constexpr int NCS = NCS_, CSM = CSM_, NTS = NTS_;
  f32x4 cs[NCS_ ? NCS_ : 1], ts[NTS_ ? NTS_ : 1], tq[NTS_ ? NTS_ : 1];
};
template <class C, int NBW, bool SWAP, bool BARR, bool RDN, class E>
DI void kstep(f32x4 (&acc)[C::MBW][NBW], E& ex, const Frags<C, NBW>& cur, Frags<C, NBW>& nxt,
              LAS const char* nslot, const CAddr<C>& a, int nks, bf16x8 ones) {
  constexpr int MBW = C::MBW, NM = MBW * NBW, NR = RD<C, NBW>::NR;
  constexpr int F0 = BARR ? MBW : 0, NSL = NM - F0, PER = (NR + NSL - 1) / NSL;
#pragma unroll
  for (int m = 0; m < NM; m++) {
    const int j = m / MBW, i = m % MBW;
    bf16x8 wf = wfrag<C, NBW>(cur, j);
    acc[i][j] = SWAP ? mfma(wf, cur.t[i], acc[i][j]) : mfma(cur.t[i], wf, acc[i][j]);
    if (i == MBW - 1 && j < E::NCS) ex.cs[j] = E::CSM ? mfma(wf, ones, ex.cs[j]) : mfma(ones, wf, ex.cs[j]);
    if (j == 2 && i < E::NTS) ex.ts[i] = mfma(ones, cur.t[i], ex.ts[i]);
    if (j == 3 && i < E::NTS) ex.tq[i] = mfma(cur.t[i], cur.t[i], ex.tq[i]);
    if (BARR && m == F0 - 1) { SB(); BAR(); }
    if (RDN && m >= F0) {
#pragma unroll
      for (int r = (m - F0) * PER; r < (m - F0 + 1) * PER && r < NR; r++) read_one<C, NBW>(nxt, nslot, a, nks, r);
    }
    SB();
  }
}
template <class C, int NBW, bool SWAP, bool LAST, class E>
DI void consume(f32x4 (&acc)[C::MBW][NBW], E& ex, Frags<C, NBW>& fa, LAS const char* lds, const CAddr<C>& a, int s0, int s1, bf16x8 ones) {
  Frags<C, NBW> fb;
  const int e = LAST ? s1 - 1 : s1;
#pragma unroll 1
  for (int s = s0; s < e; s++) {
    LAS const char* slot = lds + (s & 3) * C::STAGE;
    LAS const char* nsl = lds + ((s + 1) & 3) * C::STAGE;
#pragma unroll
    for (int ks = 0; ks < C::KS; ks += 2) {
      kstep<C, NBW, SWAP, false, true>(acc, ex, fa, fb, slot, a, ks + 1, ones);
      if (ks + 2 < C::KS) kstep<C, NBW, SWAP, false, true>(acc, ex, fb, fa, slot, a, ks + 2, ones);
      else kstep<C, NBW, SWAP, true, true>(acc, ex, fb, fa, nsl, a, 0, ones);
    }
  }
  if (LAST) {
    LAS const char* slot = lds + (e & 3) * C::STAGE;
#pragma unroll
    for (int ks = 0; ks < C::KS; ks += 2) {
      kstep<C, NBW, SWAP, false, true>(acc, ex, fa, fb, slot, a, ks + 1, ones);
      if (ks + 2 < C::KS) kstep<C, NBW, SWAP, false, true>(acc, ex, fb, fa, slot, a, ks + 2, ones);
      else kstep<C, NBW, SWAP, false, false>(acc, ex, fb, fa, slot, a, 0, ones);
    }
  }
}
template <int A, int B> DI void zero(f32x4 (&acc)[A][B]) {
#pragma unroll
  for (int i = 0; i < A; i++)
#pragma unroll
    for (int j = 0; j < B; j++) acc[i][j] = f32x4{0.f, 0.f, 0.f, 0.f};
}
template <int A, int M, int B> DI void zero(Extra<A, M, B>& e) {
#pragma unroll
  for (int i = 0; i < (A ? A : 1); i++) e.cs[i] = f32x4{0.f, 0.f, 0.f, 0.f};
#pragma unroll
  for (int i = 0; i < (B ? B : 1); i++) e.ts[i] = e.tq[i] = f32x4{0.f, 0.f, 0.f, 0.f};
}
DI bf16x8 ones_frag() { u32x4 o = {0x3f803f80u, 0x3f803f80u, 0x3f803f80u, 0x3f803f80u}; return __builtin_bit_cast(bf16x8, o); }
#define CBAR() asm volatile("s_waitcnt lgkmcnt(0)\n\ts_barrier" ::: "memory")

extern __shared__ __attribute__((aligned(16))) char smem[];

#define AUXF ((LAS float*)(lds + LDS_AUX))
DI f32x4 ldsf4(LAS const float* p) { return *(LAS const f32x4*)p; }
DI float sel4(f32x4 v, int k) { float a = (k & 1) ? v[1] : v[0], b = (k & 1) ? v[3] : v[2]; return (k & 2) ? b : a; }

__global__ __launch_bounds__(512) void phase1(const char* __restrict__ x, const char* __restrict__ wqkv, const char* __restrict__ w1, char* __restrict__ ws) {
  typedef CfgA1 C;
  const int b = blockIdx.x, xcd = b & 7, idx = b >> 3;
  const int rt = (xcd >> 1) * 4 + (idx & 3), ct = (xcd & 1) * 8 + (idx >> 2);
  if (ct >= 15) return;
  const int wid = __builtin_amdgcn_readfirstlane(threadIdx.x >> 6), lane = threadIdx.x & 63;
  LAS char* lds = (LAS char*)smem;
  STAMP(0, 0);
  if (wid >= 4) {
    const int w = wid - 4;
    unsigned tv[C::PT], wv[C::PW];
    const char* wb = ct < 10 ? wqkv + (640 + ct * 128) * 2 : w1 + (ct - 10) * 256;
    const int pitch = ct < 10 ? 3840 : 1280;
    make_tokv<C>(tv, w, lane, 0); make_wv<C>(wv, w, lane, pitch);
    loader<C>(w, (unsigned)(size_t)lds, x + (size_t)rt * 128 * 1280, 128, tv, wb, 64 * pitch, wv, 10);
    STAMP(0, 1);
    return;
  }
  const int wm = wid >> 1, wn = wid & 1, g = lane >> 4, tk = lane & 15;
  CAddr<C> a; make_caddr<C>(a, wm, wn, lane, 2 * wn, 2 * wm);
  f32x4 acc[4][4]; zero(acc);
  Extra<2, 0, 2> ex; zero(ex);
  const bf16x8 ones = ones_frag();
  Frags<C, 4> fa;
  CBAR(); STAMP(0, 1); load_all<C, 4>(fa, lds, a, 0);
  if (ct < 10) consume<C, 4, false, true>(acc, ex, fa, lds, a, 0, 10, ones);
  else consume<C, 4, true, true>(acc, ex, fa, lds, a, 0, 10, ones);
  STAMP(0, 2);
  {
#pragma unroll
    for (int i = 0; i < 2; i++) {
      float m = ex.ts[i][0] * (1.0f / D_), q = sel4(ex.tq[i], tk & 3);
      float var = fmaxf(q * (1.0f / D_) - m * m, 0.f);
      float rs = __builtin_amdgcn_rsqf(var + EPS);
      float r = rs;
      if (ct >= 10) r = rs * __builtin_amdgcn_rsqf(var * rs * rs + EPS);
      int row = wm * 64 + (2 * wn + i) * 16 + tk;
      if (g == (tk >> 2)) {
        AUXF[row] = r; AUXF[128 + row] = r * m;
        if (ct == 0) ((f32x2*)(ws + OFF_ST))[rt * 128 + row] = f32x2{m, rs};
      }
      if (g == 0) AUXF[256 + wn * 64 + (2 * wm + i) * 16 + tk] = ex.cs[i][0];
    }
  }
  CBAR();
  STAMP(0, 3);
  if (ct < 10) {
    char* dst = ws + (ct < 5 ? OFF_PK : OFF_V) + lane * 16;
    const int cb0 = (ct < 5 ? ct : ct - 5) * 8 + wn * 4;
    float cs[4]; f32x2 zs[4];
#pragma unroll
    for (int j = 0; j < 4; j++) { cs[j] = AUXF[256 + wn * 64 + ((j + 2 * wm) & 3) * 16 + tk]; zs[j] = f32x2{0.f, 0.f}; }
#pragma unroll
    for (int ip = 0; ip < 2; ip++) {
      const int tb = wm * 64 + (ip ^ wn) * 32 + 4 * g;
      f32x4 r0 = ldsf4(AUXF + tb), r1 = ldsf4(AUXF + tb + 16), m0 = ldsf4(AUXF + 128 + tb), m1 = ldsf4(AUXF + 128 + tb + 16);
      char* d2 = dst + (size_t)((rt * 4 + wm * 2 + (ip ^ wn)) * 40 + cb0) * 1024;
#pragma unroll
      for (int j = 0; j < 4; j++) {
        f32x4 t0 = r0 * acc[2 * ip][j] - m0 * cs[j], t1 = r1 * acc[2 * ip + 1][j] - m1 * cs[j];
        u32x4 o;
        if (ct < 5) {
          o = u32x4{pk2v(phi2(lo2(t0))), pk2v(phi2(hi2(t0))), pk2v(phi2(lo2(t1))), pk2v(phi2(hi2(t1)))};
          zs[j] += (unpk(o[0]) + unpk(o[1])) + (unpk(o[2]) + unpk(o[3]));
        } else {
          o = u32x4{pk2v(lo2(t0)), pk2v(hi2(t0)), pk2v(lo2(t1)), pk2v(hi2(t1))};
        }
        *(u32x4*)(d2 + ((j + 2 * wm) & 3) * 1024) = o;
      }
    }
    if (ct < 5) {
      float z[4];
#pragma unroll
      for (int j = 0; j < 4; j++) { z[j] = zs[j][0] + zs[j][1]; z[j] += shx(z[j], 16); z[j] += shx(z[j], 32); }
      if (wm == 1 && lane < 16) {
#pragma unroll
        for (int j = 0; j < 4; j++) AUXF[704 + wn * 64 + ((j + 2) & 3) * 16 + lane] = z[j];
      }
      CBAR();
      if (wm == 0 && lane < 16) {
        float* zp = (float*)(ws + OFF_ZP) + rt * 640 + ct * 128 + wn * 64;
#pragma unroll
        for (int j = 0; j < 4; j++) zp[j * 16 + lane] = z[j] + AUXF[704 + wn * 64 + j * 16 + lane];
      }
    }
  } else {
    const int c0 = (ct - 10) * 128 + wn * 64;
#pragma unroll
    for (int i = 0; i < 4; i++) {
      const int row = wm * 64 + ((i + 2 * wn) & 3) * 16 + tk;
      const float rr = AUXF[row], rm = AUXF[128 + row];
#pragma unroll
      for (int jp = 0; jp < 2; jp++) {
        const int jt = jp ^ wm;
        u32x4 o;
#pragma unroll
        for (int h = 0; h < 2; h++) {
          f32x4 cs = ldsf4(AUXF + 256 + wn * 64 + (2 * jt + h) * 16 + 4 * g);
          f32x4 t = rr * acc[i][2 * jp + h] - rm * cs;
          o[2 * h] = pk2v(gelu2(lo2(t))); o[2 * h + 1] = pk2v(gelu2(hi2(t)));
        }
        const int col = c0 + jt * 32, grp = col / 160;
        const int dl = grp == 0 ? 1 : (grp == 1 ? -1 : (grp == 2 ? 2 : -2));
        int td = rt * 128 + row + dl;
        const bool ok = td >= 0 && td < S_;
        td &= S_ - 1;
        if (!ok) o = u32x4{0, 0, 0, 0};
        *(u32x4*)(ws + OFF_H + ((size_t)((td >> 4) * 20 + (col >> 5)) * 64 + g * 16 + (td & 15)) * 16) = o;
      }
    }
  }
  STAMPEND(0);
}

__global__ __launch_bounds__(512) void phase2(const char* __restrict__ x, const char* __restrict__ wqkv, const char* __restrict__ w2, char* __restrict__ ws) {
  const int b = blockIdx.x;
  const int wid = __builtin_amdgcn_readfirstlane(threadIdx.x >> 6), lane = threadIdx.x & 63;
  LAS char* lds = (LAS char*)smem;
  const int g = lane >> 4, tk = lane & 15;
  STAMP(1, 0);
  if (b < 128) {
    typedef CfgQ C;
    const int rt = b >> 2, ct = b & 3;
    if (wid >= 4) {
      const int w = wid - 4;
      unsigned tv[C::PT], wv[C::PW];
      make_tokv<C>(tv, w, lane, 0); make_wv<C>(wv, w, lane, 3840);
      loader<C>(w, (unsigned)(size_t)lds, x + (size_t)rt * 64 * 1280, 128, tv, wqkv + ct * 320, 64 * 3840, wv, 10);
      STAMP(1, 1);
      return;
    }
    const int wm = wid >> 1, wn = wid & 1;
    float zp[16];
    const int t = threadIdx.x;
    if (t < 160) {
      const float* p = (const float*)(ws + OFF_ZP) + ct * 160 + t;
#pragma unroll
      for (int r = 0; r < 16; r++) zp[r] = p[r * 640];
    }
    f32x2 st[2];
#pragma unroll
    for (int i = 0; i < 2; i++) st[i] = ((const f32x2*)(ws + OFF_ST))[rt * 64 + wm * 32 + i * 16 + tk];
    CAddr<C> a; make_caddr<C>(a, wm, wn, lane);
    f32x4 acc[2][5]; zero(acc);
    Extra<5, 1, 0> ex; zero(ex);
    const bf16x8 ones = ones_frag();
    Frags<C, 5> fa;
    CBAR(); STAMP(1, 1); load_all<C, 5>(fa, lds, a, 0);
    if (t < 160) {
      float z = 0.f;
#pragma unroll
      for (int r = 0; r < 16; r += 4) z += (zp[r] + zp[r + 1]) + (zp[r + 2] + zp[r + 3]);
      AUXF[512 + t] = z;
    }
    consume<C, 5, true, true>(acc, ex, fa, lds, a, 0, 10, ones);
    STAMP(1, 2);
#pragma unroll
    for (int i = 0; i < 2; i++) {
      const float rs = st[i][1], rm = rs * st[i][0];
      f32x4 pq[5]; float part[5];
#pragma unroll
      for (int j = 0; j < 5; j++) {
        f32x4 z = ldsf4(AUXF + 512 + wn * 80 + j * 16 + 4 * g);
        f32x4 tt = rs * acc[i][j] - rm * ex.cs[j];
        f32x2 p0 = phi2(lo2(tt)), p1 = phi2(hi2(tt));
        pq[j] = f32x4{p0[0], p0[1], p1[0], p1[1]};
        f32x2 s = p0 * lo2(z) + p1 * hi2(z);
        part[j] = s[0] + s[1];
      }
      float dh[4];
      dh[0] = part[0] + (g == 0 ? part[1] : 0.f);
      dh[1] = (g >= 1 ? part[1] : 0.f) + (g <= 1 ? part[2] : 0.f);
      dh[2] = (g >= 2 ? part[2] : 0.f) + (g <= 2 ? part[3] : 0.f);
      dh[3] = (g == 3 ? part[3] : 0.f) + part[4];
#pragma unroll
      for (int h = 0; h < 4; h++) { dh[h] += shx(dh[h], 16); dh[h] += shx(dh[h], 32); dh[h] = rcp(dh[h] + 1e-6f); }
      const float sc[5] = {dh[0], g == 0 ? dh[0] : dh[1], g <= 1 ? dh[1] : dh[2], g <= 2 ? dh[2] : dh[3], dh[3]};
#pragma unroll
      for (int j = 0; j < 5; j++) pq[j] *= sc[j];
      char* dst = ws + OFF_HP + ((size_t)(rt * 4 + wm * 2 + i) * 68 + 48 + ct * 5) * 1024 + lane * 16;
      *(u32x4*)(dst + (3 * wn) * 1024) = u32x4{pk2v(lo2(pq[0])), pk2v(hi2(pq[0])), pk2v(lo2(pq[1])), pk2v(hi2(pq[1]))};
      *(u32x4*)(dst + (3 * wn + 1) * 1024) = u32x4{pk2v(lo2(pq[2])), pk2v(hi2(pq[2])), pk2v(lo2(pq[3])), pk2v(hi2(pq[3]))};
      *(u32x2*)(dst + 2 * 1024 + wn * 8) = u32x2{pk2v(lo2(pq[4])), pk2v(hi2(pq[4]))};
    }
    STAMPEND(1);
  } else if (b < 208) {
    typedef CfgA C;
    const int bb = b - 128, rt = (bb & 7) * 2 + (bb >> 3) / 5, ct = (bb >> 3) % 5;
    if (wid >= 4) {
      const int w = wid - 4;
      unsigned tv[C::PT], wv[C::PW];
      make_tokv<C>(tv, w, lane, 20); make_wv<C>(wv, w, lane, 1280);
      loader<C>(w, (unsigned)(size_t)lds, ws + OFF_H + (size_t)rt * 8 * 20 * 1024, 2048, tv, w2 + ct * 256, 64 * 1280, wv, 10);
      STAMP(1, 1);
      return;
    }
    const int wm = wid >> 1, wn = wid & 1;
    u32x2 xv[4][4]; f32x2 st[4];
#pragma unroll
    for (int i = 0; i < 4; i++) {
      int row = rt * 128 + wm * 64 + i * 16 + tk;
      st[i] = ((const f32x2*)(ws + OFF_ST))[row];
#pragma unroll
      for (int j = 0; j < 4; j++) xv[i][j] = *(const u32x2*)(x + (size_t)row * 1280 + (ct * 128 + wn * 64 + j * 16 + 4 * g) * 2);
    }
    CAddr<C> a; make_caddr<C>(a, wm, wn, lane);
    f32x4 acc[4][4]; zero(acc);
    Extra<0, 0, 0> ex;
    const bf16x8 ones = ones_frag();
    Frags<C, 4> fa;
    CBAR(); STAMP(1, 1); load_all<C, 4>(fa, lds, a, 0);
    consume<C, 4, true, true>(acc, ex, fa, lds, a, 0, 10, ones);
    STAMP(1, 2);
#pragma unroll
    for (int i = 0; i < 4; i++) {
      const float rs = st[i][1], rm = st[i][0] * rs;
      f32x2 s1 = {0.f, 0.f}, s2 = {0.f, 0.f};
      char* dst = ws + OFF_X1 + ((size_t)(rt * 8 + wm * 4 + i) * 20 + ct * 4 + wn * 2) * 1024 + lane * 16;
#pragma unroll
      for (int jp = 0; jp < 2; jp++) {
        u32x4 o;
#pragma unroll
        for (int h = 0; h < 2; h++) {
          u32x2 xx = xv[i][2 * jp + h]; f32x4 c = acc[i][2 * jp + h] - rm;
          o[2 * h] = pk2v(unpk(xx[0]) * rs + lo2(c));
          o[2 * h + 1] = pk2v(unpk(xx[1]) * rs + hi2(c));
        }
#pragma unroll
        for (int e = 0; e < 4; e++) { f32x2 v = unpk(o[e]); s1 += v; s2 += v * v; }
        *(u32x4*)(dst + jp * 1024) = o;
      }
      float a1 = s1[0] + s1[1], a2 = s2[0] + s2[1];
      a1 += shx(a1, 16); a2 += shx(a2, 16); a1 += shx(a1, 32); a2 += shx(a2, 32);
      if (g == 0) ((f32x2*)(ws + OFF_XS))[(size_t)(rt * 128 + wm * 64 + i * 16 + tk) * 10 + ct * 2 + wn] = f32x2{a1, a2};
    }
    STAMPEND(1);
  } else if (b < 240) {
    const int bb = b - 208, G = bb & 7, R = bb >> 3;
    f32x4 acc[13];
#pragma unroll
    for (int i = 0; i < 13; i++) acc[i] = f32x4{0.f, 0.f, 0.f, 0.f};
    bf16x8 pk[2][5], vv[2][5];
#pragma unroll
    for (int u = 0; u < 2; u++)
#pragma unroll
      for (int j = 0; j < 5; j++) {
        size_t o = ((size_t)(R * 16 + wid * 2 + u) * 40 + G * 5 + j) * 1024 + lane * 16;
        pk[u][j] = *(const bf16x8*)(ws + OFF_PK + o); vv[u][j] = *(const bf16x8*)(ws + OFF_V + o);
      }
#pragma unroll
    for (int u = 0; u < 2; u++)
#pragma unroll
      for (int db = 0; db < 5; db++)
#pragma unroll
        for (int eb = db - 1; eb <= db + 1; eb++)
          if (eb >= 0 && eb < 5) acc[2 * db + eb] = mfma(pk[u][db], vv[u][eb], acc[2 * db + eb]);
    STAMP(1, 2);
#pragma unroll
    for (int i = 0; i < 13; i++) *(LAS f32x4*)(lds + (wid * 13 + i) * 1024 + lane * 16) = acc[i];
    __syncthreads();
    for (int o = threadIdx.x; o < 832; o += 512) {
      f32x4 s = *(LAS const f32x4*)(lds + o * 16);
#pragma unroll
      for (int w = 1; w < 8; w++) s += *(LAS const f32x4*)(lds + w * 13 * 1024 + o * 16);
      *(f32x4*)(ws + OFF_KVP + (size_t)(R * 8 + G) * 13 * 1024 + o * 16) = s;
    }
    STAMPEND(1);
  }
}

__global__ __launch_bounds__(512) void phase3(const char* __restrict__ wo, const char* __restrict__ w3, const char* __restrict__ w4, char* __restrict__ ws) {
  const int b = blockIdx.x;
  const int wid = __builtin_amdgcn_readfirstlane(threadIdx.x >> 6), lane = threadIdx.x & 63;
  LAS char* lds = (LAS char*)smem;
  STAMP(2, 0);
  if (b < 192) {
    typedef CfgA C;
    const int xcd = b & 7, idx = b >> 3;
    const int rt = (xcd >> 1) * 4 + (idx & 3), ct = (xcd & 1) * 6 + (idx >> 2);
    if (wid >= 4) {
      const int w = wid - 4;
      unsigned tv[C::PT], wv[C::PW];
      make_tokv<C>(tv, w, lane, 20); make_wv<C>(wv, w, lane, 3072);
      loader<C>(w, (unsigned)(size_t)lds, ws + OFF_X1 + (size_t)rt * 8 * 20 * 1024, 2048, tv, w3 + ct * 256, 64 * 3072, wv, 10);
      STAMP(2, 1);
      return;
    }
    const int wm = wid >> 1, wn = wid & 1, g = lane >> 4, tk = lane & 15;
    f32x4 xs[5];
    if (wid < 2) {
      const f32x4* p = (const f32x4*)(ws + OFF_XS + (size_t)(rt * 128 + threadIdx.x) * 80);
#pragma unroll
      for (int i = 0; i < 5; i++) xs[i] = p[i];
    }
    CAddr<C> a; make_caddr<C>(a, wm, wn, lane);
    f32x4 acc[4][4]; zero(acc);
    Extra<4, 1, 0> ex; zero(ex);
    const bf16x8 ones = ones_frag();
    Frags<C, 4> fa;
    CBAR(); STAMP(2, 1); load_all<C, 4>(fa, lds, a, 0);
    if (wid < 2) {
      f32x4 s = (xs[0] + xs[1]) + (xs[2] + xs[3]) + xs[4];
      float m = (s[0] + s[2]) * (1.0f / D_), var = fmaxf((s[1] + s[3]) * (1.0f / D_) - m * m, 0.f);
      float rs = __builtin_amdgcn_rsqf(var + EPS);
      AUXF[threadIdx.x] = rs; AUXF[128 + threadIdx.x] = rs * m;
    }
    consume<C, 4, true, true>(acc, ex, fa, lds, a, 0, 10, ones);
    STAMP(2, 2);
#pragma unroll
    for (int i = 0; i < 4; i++) {
      const int row = wm * 64 + i * 16 + tk;
      const float rs = AUXF[row], rm = AUXF[128 + row];
      char* dst = ws + OFF_HP + ((size_t)(rt * 8 + wm * 4 + i) * 68 + ct * 4 + wn * 2) * 1024 + lane * 16;
#pragma unroll
      for (int jp = 0; jp < 2; jp++) {
        u32x4 o;
#pragma unroll
        for (int h = 0; h < 2; h++) {
          f32x4 t = rs * acc[i][2 * jp + h] - rm * ex.cs[2 * jp + h];
          o[2 * h] = pk2v(gelu2(lo2(t))); o[2 * h + 1] = pk2v(gelu2(hi2(t)));
        }
        *(u32x4*)(dst + jp * 1024) = o;
      }
    }
    STAMPEND(2);
  } else {
    const int bb = b - 192, h = bb >> 1, half = bb & 1, t = threadIdx.x;
    LAS float* kv = (LAS float*)lds;
    LAS char* wt = lds + 2048;
    u32x4 wl[4];
#pragma unroll
    for (int i = 0; i < 4; i++) {
      int s = i * 512 + t;
      if (s < 1920) wl[i] = *(const u32x4*)(w4 + (size_t)(96 * (bb >> 2) + s / 20) * 1280 + (bb & 3) * 320 + (s % 20) * 16);
    }
    float kvv = 0.f;
    if (t < 400) {
      int d = t % 20, e = t / 20;
      int hh = h & 3, G = h >> 2, da = 20 * hh + d, ea = 20 * hh + e;
      int fi = (2 * (da >> 4) + (ea >> 4)) * 256 + (((da & 15) >> 2) * 16 + (ea & 15)) * 4 + (da & 3);
      const float* p = (const float*)(ws + OFF_KVP) + G * 13 * 256 + fi;
      kvv = (p[0] + p[8 * 13 * 256]) + (p[2 * 8 * 13 * 256] + p[3 * 8 * 13 * 256]);
    }
    const int cp = t % 160, dg = t / 160;
    unsigned wv[20];
    if (dg < 3) {
#pragma unroll
      for (int e = 0; e < 20; e++) wv[e] = *(const unsigned*)(wo + (size_t)(20 * h + e) * 1280 + (half * 320 + cp * 2) * 2);
    }
    if (t < 400) kv[t] = kvv;
#pragma unroll
    for (int i = 0; i < 4; i++) {
      int s = i * 512 + t;
      if (s < 1920) *(LAS u32x4*)(wt + s * 16) = wl[i];
    }
    __syncthreads();
    {
      const int gq = lane >> 4, q = (lane >> 2) & 3, p = lane & 3;
      for (int f = wid; f < 30; f += 8) {
        int ks = f / 10, nb = f % 10;
        LAS const char* src = wt + (ks * 32 + 4 * gq + q) * 320 + nb * 32 + p * 8;
        bf16x8 v = cat(ldstr(src), ldstr(src + 16 * 320));
        *(bf16x8*)(ws + OFF_WT + ((size_t)(3 * (bb >> 2) + ks) * 40 + (bb & 3) * 10 + nb) * 1024 + lane * 16) = v;
      }
    }
    if (dg < 3) {
      f32x4 a0[2], a1[2];
#pragma unroll
      for (int u = 0; u < 2; u++) a0[u] = a1[u] = f32x4{0.f, 0.f, 0.f, 0.f};
#pragma unroll
      for (int e = 0; e < 20; e++) {
        const float wl_ = bflo(wv[e]), wh_ = bfhi(wv[e]);
#pragma unroll
        for (int u = 0; u < 2; u++) { f32x4 k = ldsf4(kv + e * 20 + 8 * dg + 4 * u); a0[u] += k * wl_; a1[u] += k * wh_; }
      }
#pragma unroll
      for (int u = 0; u < 2; u++) {
        const int q = dg * 2 + u;
        if (q < 5) {
          int c = 20 * h + 4 * q, beta = c >> 4, G = beta / 10, j = beta % 10, wn = j / 5, jj = j % 5;
          int kap = jj == 4 ? 5 * G + 2 : 5 * G + 3 * wn + (jj >> 1), tt = jj == 4 ? wn : (jj & 1);
          int n = half * 320 + cp * 2;
          char* dst = ws + OFF_WT + ((size_t)(48 + kap) * 40 + (n >> 4)) * 1024 + (((c & 15) >> 2) * 16 + (n & 15)) * 16 + tt * 8;
          *(u32x2*)dst = u32x2{pk2v(lo2(a0[u])), pk2v(hi2(a0[u]))};
          *(u32x2*)(dst + 16) = u32x2{pk2v(lo2(a1[u])), pk2v(hi2(a1[u]))};
        }
      }
    }
    STAMPEND(2);
  }
}

template <int NBW>
DI void p4_consumer(const char* __restrict__ x, char* __restrict__ out, char* __restrict__ ws, LAS char* lds, int rt, int ct, int wm, int wn, int lane) {
  typedef CfgC C;
  const int g = lane >> 4, tk = lane & 15;
  u32x2 xv[2][NBW], x1v[2][NBW];
#pragma unroll
  for (int i = 0; i < 2; i++) {
    int row = rt * 64 + wm * 32 + i * 16 + tk;
#pragma unroll
    for (int j = 0; j < NBW; j++) {
      int blk = ct * 5 + wn * 3 + j;
      xv[i][j] = *(const u32x2*)(x + (size_t)row * 1280 + (blk * 16 + 4 * g) * 2);
      x1v[i][j] = *(const u32x2*)(ws + OFF_X1 + ((size_t)(row >> 4) * 20 + (blk >> 1)) * 1024 + lane * 16 + (blk & 1) * 8);
    }
  }
  CAddr<C> a; make_caddr<C>(a, wm, wn, lane);
  f32x4 h2[2][NBW], sa[2][NBW]; zero(h2); zero(sa);
  Extra<0, 0, 0> ex;
  const bf16x8 ones = ones_frag();
  Frags<C, NBW> fa;
  CBAR(); STAMP(3, 1); load_all<C, NBW>(fa, lds, a, 0);
  consume<C, NBW, true, false>(h2, ex, fa, lds, a, 0, 12, ones);
  consume<C, NBW, true, true>(sa, ex, fa, lds, a, 12, 17, ones);
  STAMP(3, 2);
#pragma unroll
  for (int i = 0; i < 2; i++) {
    int row = rt * 64 + wm * 32 + i * 16 + tk;
#pragma unroll
    for (int j = 0; j < NBW; j++) {
      int blk = ct * 5 + wn * 3 + j;
      u32x2 o;
#pragma unroll
      for (int e = 0; e < 2; e++) {
        f32x2 bb = unpk(x1v[i][j][e]) + (e ? hi2(h2[i][j]) : lo2(h2[i][j]));
        f32x2 z = bb * -1.4426950408889634f;
        f32x2 d = f32x2{ex2(z[0]), ex2(z[1])} + 1.0f;
        f32x2 sl = bb * f32x2{rcp(d[0]), rcp(d[1])};
        o[e] = pk2v(unpk(xv[i][j][e]) + (e ? hi2(sa[i][j]) : lo2(sa[i][j])) * sl);
      }
      *(u32x2*)(out + (size_t)row * 1280 + (blk * 16 + 4 * g) * 2) = o;
    }
  }
  STAMPEND(3);
}
__global__ __launch_bounds__(512) void phase4(const char* __restrict__ x, char* __restrict__ ws, char* __restrict__ out) {
  typedef CfgC C;
  const int b = blockIdx.x, xcd = b & 7, idx = b >> 3;
  const int rt = (xcd >> 1) * 8 + (idx & 7), ct = (xcd & 1) * 4 + (idx >> 3);
  const int wid = __builtin_amdgcn_readfirstlane(threadIdx.x >> 6), lane = threadIdx.x & 63;
  LAS char* lds = (LAS char*)smem;
  STAMP(3, 0);
  if (wid >= 4) {
    const int w = wid - 4;
    unsigned tv[C::PT], wv[C::PW];
    make_tokv<C>(tv, w, lane, 68); make_wv<C>(wv, w, lane, 0);
    loader<C>(w, (unsigned)(size_t)lds, ws + OFF_HP + (size_t)rt * 4 * 68 * 1024, 4096, tv, ws + OFF_WT + ct * 5 * 1024, 4 * 40 * 1024, wv, 17);
    STAMP(3, 1);
    return;
  }
  const int wm = wid >> 1, wn = wid & 1;
  if (wn == 0) p4_consumer<3>(x, out, ws, lds, rt, ct, wm, wn, lane);
  else p4_consumer<2>(x, out, ws, lds, rt, ct, wm, wn, lane);
}

extern "C" void kernel_launch(void* const* d_in, const int* in_sizes, int n_in, void* d_out, int out_size, void* d_ws, size_t ws_size, hipStream_t stream) {
  const char* x = (const char*)d_in[0]; const char* wqkv = (const char*)d_in[1]; const char* wo = (const char*)d_in[2];
  const char* w1 = (const char*)d_in[4]; const char* w2 = (const char*)d_in[5]; const char* w3 = (const char*)d_in[6]; const char* w4 = (const char*)d_in[7];
  char* ws = (char*)d_ws;
  (void)hipFuncSetAttribute((const void*)phase1, hipFuncAttributeMaxDynamicSharedMemorySize, LDS_TOTAL);
  (void)hipFuncSetAttribute((const void*)phase2, hipFuncAttributeMaxDynamicSharedMemorySize, LDS_TOTAL);
  (void)hipFuncSetAttribute((const void*)phase3, hipFuncAttributeMaxDynamicSharedMemorySize, LDS_TOTAL);
  (void)hipFuncSetAttribute((const void*)phase4, hipFuncAttributeMaxDynamicSharedMemorySize, LDS_TOTAL);
  phase1<<<256, 512, LDS_TOTAL, stream>>>(x, wqkv, w1, ws);
  phase2<<<240, 512, LDS_TOTAL, stream>>>(x, wqkv, w2, ws);
  phase3<<<256, 512, LDS_TOTAL, stream>>>(wo, w3, w4, ws);
  phase4<<<256, 512, LDS_TOTAL, stream>>>(x, ws, (char*)d_out);
}
```

```cpp
#include <hip/hip_runtime.h>
#include <stdint.h>

typedef __attribute__((ext_vector_type(8))) __bf16 bf16x8;
typedef __attribute__((ext_vector_type(2))) __bf16 bf16x2;
typedef __attribute__((ext_vector_type(4))) float f32x4;
typedef __attribute__((ext_vector_type(2))) float f32x2;
typedef __attribute__((ext_vector_type(4))) short v4i16;
typedef __attribute__((ext_vector_type(8))) short v8i16;
typedef __attribute__((ext_vector_type(4))) unsigned u32x4;
typedef __attribute__((ext_vector_type(2))) unsigned u32x2;
#define LAS __attribute__((address_space(3)))
#define DI __device__ __forceinline__
#define SB() __builtin_amdgcn_sched_barrier(0)

constexpr int S_ = 2048, D_ = 640, F_ = 1536;
constexpr size_t OFF_PK = 0;
constexpr size_t OFF_V = OFF_PK + 64 * 40 * 1024;
constexpr size_t OFF_H = OFF_V + 64 * 40 * 1024;
constexpr size_t OFF_X1 = OFF_H + 128 * 20 * 1024;
constexpr size_t OFF_HP = OFF_X1 + 128 * 20 * 1024;
constexpr size_t OFF_WT = OFF_HP + 128 * 68 * 1024;
constexpr size_t OFF_ZP = OFF_WT + 68 * 40 * 1024;
constexpr size_t OFF_ST = OFF_ZP + 16 * 640 * 4;
constexpr size_t OFF_XS = OFF_ST + 2048 * 8;
constexpr size_t OFF_KVP = OFF_XS + 2048 * 80;
constexpr float EPS = 1e-5f;
#define STAMP(k, i)
#define STAMPEND(k)
#define CSTAMP(k, i)

DI void dma16(const void* sbase, unsigned voff, unsigned ldsdst) {
  unsigned keep;
  asm volatile("s_mov_b32 %0, m0\n\ts_mov_b32 m0, %3\n\ts_nop 0\n\tglobal_load_lds_dwordx4 %1, %2\n\ts_mov_b32 m0, %0"
               : "=&s"(keep) : "v"(voff), "s"(sbase), "s"(ldsdst) : "memory");
}
DI void dma4(const void* sbase, unsigned voff, unsigned ldsdst) {
  unsigned keep;
  asm volatile("s_mov_b32 %0, m0\n\ts_mov_b32 m0, %3\n\ts_nop 0\n\tglobal_load_lds_dword %1, %2\n\ts_mov_b32 m0, %0"
               : "=&s"(keep) : "v"(voff), "s"(sbase), "s"(ldsdst) : "memory");
}
#define STM 0
DI void st16(void* p, u32x4 v) {
  *(u32x4*)p = v;
}
#define BAR() asm volatile("s_waitcnt lgkmcnt(0)\n\ts_barrier" ::: "memory")
template <int N> DI void waitvm() { asm volatile("s_waitcnt vmcnt(%0)" ::"n"(N) : "memory"); }
DI unsigned pk2(float lo, float hi) { f32x2 v = {lo, hi}; bf16x2 b = __builtin_convertvector(v, bf16x2); return __builtin_bit_cast(unsigned, b); }
DI unsigned pk2v(f32x2 v) { bf16x2 b = __builtin_convertvector(v, bf16x2); return __builtin_bit_cast(unsigned, b); }
DI float bflo(unsigned u) { return __uint_as_float(u << 16); }
DI float bfhi(unsigned u) { return __uint_as_float(u & 0xffff0000u); }
DI f32x2 unpk(unsigned u) { return f32x2{bflo(u), bfhi(u)}; }
DI float ex2(float x) { return __builtin_amdgcn_exp2f(x); }
DI float rcp(float x) { return __builtin_amdgcn_rcpf(x); }
DI f32x2 lo2(f32x4 v) { return f32x2{v[0], v[1]}; }
DI f32x2 hi2(f32x4 v) { return f32x2{v[2], v[3]}; }
DI f32x2 gelu2(f32x2 t) {
  const float c1 = -1.5957691216057308f * 1.4426950408889634f, c2 = c1 * 0.044715f;
  f32x2 u = t * t;
  f32x2 z = t * (u * c2 + c1);
  f32x2 d = f32x2{ex2(z[0]), ex2(z[1])} + 1.0f;
  return t * f32x2{rcp(d[0]), rcp(d[1])};
}
DI f32x2 phi2(f32x2 t) {
  f32x2 a = t + (1.0f + 1e-6f), z = t * 1.4426950408889634f;
  f32x2 e = f32x2{ex2(z[0]), ex2(z[1])} + 1e-6f;
  return f32x2{t[0] > 0.f ? a[0] : e[0], t[1] > 0.f ? a[1] : e[1]};
}
DI float shx(float v, int m) { return __shfl_xor(v, m, 64); }
DI bf16x8 lds128(LAS const char* p) { return *(LAS const bf16x8*)p; }
DI v4i16 ldstr(LAS const char* p) { return __builtin_amdgcn_ds_read_tr16_b64_v4i16((LAS v4i16*)p); }
DI bf16x8 cat(v4i16 a, v4i16 b) { v8i16 r = __builtin_shufflevector(a, b, 0, 1, 2, 3, 4, 5, 6, 7); return __builtin_bit_cast(bf16x8, r); }
DI f32x4 mfma(bf16x8 a, bf16x8 b, f32x4 c) { return __builtin_amdgcn_mfma_f32_16x16x32_bf16(a, b, c, 0, 0, 0); }

template <int MB_, int NB_, int NB0_, int KS_, int D_R, bool XROW_, bool FRAGW_>
struct Cfg {
  static constexpr int MB = MB_, NB = NB_, NB0 = NB0_, KS = KS_, D = D_R;
  static constexpr bool XROW = XROW_, FRAGW = FRAGW_;
  static constexpr int MBW = MB / 2, RB = NB * 32, TOKB = MB * KS * 1024, WB = KS * 32 * RB, STAGE = TOKB + WB;
  static constexpr int PT = TOKB / 4096, PW = WB / 4096, P = PT + PW;
};
typedef Cfg<8, 8, 4, 2, 4, true, false> CfgA1;
typedef Cfg<8, 8, 4, 2, 4, false, false> CfgA;
typedef Cfg<4, 10, 5, 2, 4, true, false> CfgQ;
typedef Cfg<4, 5, 3, 4, 4, false, true> CfgC;
constexpr int LDS_AUX = 147456;
constexpr int LDS_TOTAL = LDS_AUX + 4096;

template <int RB> DI unsigned wswz(unsigned rho) { return RB == 256 ? 2 * (rho & 7) : (RB == 320 ? 2 * ((rho >> 2) & 1) : 0); }

template <class C, int XT = 0>
DI void loader(int w, unsigned lds0, const char* tokbase, int tokstride, const unsigned (&tokv)[C::PT],
               const char* wb, int wstride, const unsigned (&wv)[C::PW], int NS, int rot = 0, const char* touch = nullptr, unsigned tvo = 0) {
  static_assert(C::D == 4, "ring depth");
  auto issue = [&](int s) {
    int sr = s + rot; sr -= sr >= NS ? NS : 0;
    const char* tb = tokbase + (size_t)sr * tokstride;
    const char* wp = wb + (size_t)sr * wstride;
    unsigned dst = lds0 + (s & 3) * C::STAGE + w * 1024;
#pragma unroll
    for (int i = 0; i < C::PT; i++) dma16(tb, tokv[i], dst + i * 4096);
#pragma unroll
    for (int i = 0; i < C::PW; i++) dma16(wp, wv[i], dst + C::TOKB + i * 4096);
  };
#define NPRE 2
  if (NPRE == 2) {
    issue(0); issue(1);
    if (XT) dma4(touch, tvo, lds0 + LDS_AUX + 3584);
    waitvm<C::P + XT>(); BAR();
    issue(2); issue(3);
    waitvm<2 * C::P + XT>(); BAR();
  } else if (NPRE == 3) {
    issue(0); issue(1); issue(2);
    waitvm<2 * C::P>(); BAR();
    issue(3);
    waitvm<2 * C::P>(); BAR();
  } else {
    issue(0); issue(1); issue(2); issue(3);
    waitvm<3 * C::P>(); BAR();
    waitvm<2 * C::P>(); BAR();
  }
  issue(4);
#pragma unroll 1
  for (int j = 2; j < NS - 2; j++) {
    waitvm<2 * C::P>(); BAR();
    if (j + 3 < NS) issue(j + 3);
  }
  waitvm<C::P>(); BAR();
  waitvm<0>(); BAR();
}

template <class C> DI void make_wv(unsigned (&wv)[C::PW], unsigned w, unsigned lane, unsigned pitch) {
#pragma unroll
  for (int i = 0; i < C::PW; i++) {
    if (C::FRAGW) {
      unsigned pc = i * 4 + w, ks = pc / C::NB, nb = pc % C::NB;
      wv[i] = (ks * 40 + nb) * 1024 + lane * 16;
    } else {
      unsigned sidx = (i * 4 + w) * 64 + lane;
      constexpr unsigned SL = C::RB / 16;
      unsigned rho = sidx / SL, c = sidx % SL;
      unsigned k = C::XROW ? ((rho & ~31u) | (((rho >> 2) & 3) << 3) | (((rho >> 4) & 1) << 2) | (rho & 3)) : rho;
      wv[i] = k * pitch + ((c ^ wswz<C::RB>(rho)) << 4);
    }
  }
}
template <class C> DI void make_tokv(unsigned (&tv)[C::PT], unsigned w, unsigned lane, unsigned kstot) {
#pragma unroll
  for (int i = 0; i < C::PT; i++) {
    unsigned qd = i * 4 + w;
    if (C::XROW) { unsigned row = qd * 8 + (lane >> 3); tv[i] = row * 1280 + (((lane & 7) ^ ((row >> 1) & 7)) << 4); }
    else { unsigned mb = qd / C::KS, ks = qd % C::KS; tv[i] = (mb * kstot + ks) * 1024 + lane * 16; }
  }
}

template <class C, int NBW> struct Frags { bf16x8 t[C::MBW]; v4i16 wl[NBW], wh[NBW]; bf16x8 wf[NBW]; };
template <class C> struct CAddr { int tok[2]; int tokb[2]; int wa[4]; };
template <class C> DI void make_caddr(CAddr<C>& a, int wm, int wn, int lane, int pi = 0, int pj = 0) {
  const int g = lane >> 4, q = (lane >> 2) & 3, p = lane & 3, t = lane & 15;
  if (C::XROW) {
    a.tok[0] = (wm * C::MBW * 16 + t) * 128 + ((g ^ ((t >> 1) & 7)) << 4);
    a.tok[1] = (wm * C::MBW * 16 + t) * 128 + (((4 + g) ^ ((t >> 1) & 7)) << 4);
  } else { a.tok[0] = wm * C::MBW * C::KS * 1024 + lane * 16; a.tok[1] = 0; }
  {
    const int rot = pi * (C::XROW ? 2048 : C::KS * 1024);
    a.tokb[0] = a.tok[0] - rot; a.tokb[1] = a.tok[1] - rot; a.tok[0] += rot; a.tok[1] += rot;
  }
  int r0 = 4 * g + q, base = C::TOKB + r0 * C::RB + (p >> 1) * 16 + (p & 1) * 8;
  if (C::FRAGW) { a.wa[0] = C::TOKB + wn * C::NB0 * 1024 + lane * 16; a.wa[1] = a.wa[2] = a.wa[3] = 0; }
  else if (C::RB == 256) {
#pragma unroll
    for (int j = 0; j < 4; j++) a.wa[j] = base + (((wn * 4 + ((j + pj) & 3)) ^ (r0 & 7)) << 5);
  } else {
    int b = g & 1, n0 = wn * C::NB0;
    a.wa[0] = base + ((n0 ^ b) << 5);
    a.wa[1] = base + (((n0 + 1) ^ b) << 5) - 32;
    a.wa[2] = a.wa[3] = 0;
  }
}
template <class C, int NBW> struct RD { static constexpr int WR = C::FRAGW ? 1 : 2, NR = C::MBW + WR * NBW; };
template <class C, int NBW>
DI void read_one(Frags<C, NBW>& f, LAS const char* slot, const CAddr<C>& a, int ks, int r) {
  constexpr int WR = RD<C, NBW>::WR;
  int ti = -1, wj = -1, h = 0;
  if (r == 0) ti = 0;
  else if (r <= WR) { wj = 0; h = r - 1; }
  else if (r < WR + C::MBW) ti = r - WR;
  else { int q = r - C::MBW; wj = q / WR; h = q % WR; }
  if (ti >= 0) {
    const bool hi = ti >= C::MBW / 2;
    f.t[ti] = C::XROW ? lds128(slot + (hi ? a.tokb[ks & 1] : a.tok[ks & 1]) + ti * 2048)
                      : lds128(slot + (hi ? a.tokb[0] : a.tok[0]) + (ti * C::KS + ks) * 1024);
  } else if (C::FRAGW) {
    f.wf[wj] = lds128(slot + a.wa[0] + (ks * C::NB + wj) * 1024);
  } else {
    LAS const char* pw = C::RB == 256 ? slot + a.wa[wj] : slot + a.wa[wj & 1] + wj * 32;
    if (h == 0) f.wl[wj] = ldstr(pw + ks * 32 * C::RB);
    else f.wh[wj] = ldstr(pw + (ks * 32 + 16) * C::RB);
  }
}
template <class C, int NBW> DI bf16x8 wfrag(const Frags<C, NBW>& f, int j) { return C::FRAGW ? f.wf[j] : cat(f.wl[j], f.wh[j]); }
template <class C, int NBW>
DI void load_all(Frags<C, NBW>& f, LAS const char* slot, const CAddr<C>& a, int ks) {
#pragma unroll
  for (int r = 0; r < RD<C, NBW>::NR; r++) read_one<C, NBW>(f, slot, a, ks, r);
}
template <int NCS_, int CSM_, int NTS_> struct Extra {
  static constexpr int NCS = NCS_, CSM = CSM_, NTS = NTS_;
  f32x4 cs[NCS_ ? NCS_ : 1], ts[NTS_ ? NTS_ : 1], tq[NTS_ ? NTS_ : 1];
};
template <class C, int NBW, bool SWAP, bool BARR, bool RDN, class E>
DI void kstep(f32x4 (&acc)[C::MBW][NBW], E& ex, const Frags<C, NBW>& cur, Frags<C, NBW>& nxt,
              LAS const char* nslot, const CAddr<C>& a, int nks, bf16x8 ones) {
  constexpr int MBW = C::MBW, NM = MBW * NBW, NR = RD<C, NBW>::NR;
  constexpr int F0 = BARR ? MBW : 0, NSL = NM - F0, PER = (NR + NSL - 1) / NSL;
#pragma unroll
  for (int m = 0; m < NM; m++) {
    const int j = m / MBW, i = m % MBW;
    bf16x8 wf = wfrag<C, NBW>(cur, j);
    acc[i][j] = SWAP ? mfma(wf, cur.t[i], acc[i][j]) : mfma(cur.t[i], wf, acc[i][j]);
    if (i == MBW - 1 && j < E::NCS) ex.cs[j] = E::CSM ? mfma(wf, ones, ex.cs[j]) : mfma(ones, wf, ex.cs[j]);
    if (j == 2 && i < E::NTS) ex.ts[i] = mfma(ones, cur.t[i], ex.ts[i]);
    if (j == 3 && i < E::NTS) ex.tq[i] = mfma(cur.t[i], cur.t[i], ex.tq[i]);
    if (BARR && m == F0 - 1) { SB(); BAR(); }
    if (RDN && m >= F0) {
#pragma unroll
      for (int r = (m - F0) * PER; r < (m - F0 + 1) * PER && r < NR; r++) read_one<C, NBW>(nxt, nslot, a, nks, r);
    }
    SB();
  }
}
template <class C, int NBW, bool SWAP, bool LAST, class E>
DI void consume(f32x4 (&acc)[C::MBW][NBW], E& ex, Frags<C, NBW>& fa, LAS const char* lds, const CAddr<C>& a, int s0, int s1, bf16x8 ones) {
  Frags<C, NBW> fb;
  const int e = LAST ? s1 - 1 : s1;
#pragma unroll 1
  for (int s = s0; s < e; s++) {
    LAS const char* slot = lds + (s & 3) * C::STAGE;
    LAS const char* nsl = lds + ((s + 1) & 3) * C::STAGE;
#pragma unroll
    for (int ks = 0; ks < C::KS; ks += 2) {
      kstep<C, NBW, SWAP, false, true>(acc, ex, fa, fb, slot, a, ks + 1, ones);
      if (ks + 2 < C::KS) kstep<C, NBW, SWAP, false, true>(acc, ex, fb, fa, slot, a, ks + 2, ones);
      else kstep<C, NBW, SWAP, true, true>(acc, ex, fb, fa, nsl, a, 0, ones);
    }
  }
  if (LAST) {
    LAS const char* slot = lds + (e & 3) * C::STAGE;
#pragma unroll
    for (int ks = 0; ks < C::KS; ks += 2) {
      kstep<C, NBW, SWAP, false, true>(acc, ex, fa, fb, slot, a, ks + 1, ones);
      if (ks + 2 < C::KS) kstep<C, NBW, SWAP, false, true>(acc, ex, fb, fa, slot, a, ks + 2, ones);
      else kstep<C, NBW, SWAP, false, false>(acc, ex, fb, fa, slot, a, 0, ones);
    }
  }
}
template <int A, int B> DI void zero(f32x4 (&acc)[A][B]) {
#pragma unroll
  for (int i = 0; i < A; i++)
#pragma unroll
    for (int j = 0; j < B; j++) acc[i][j] = f32x4{0.f, 0.f, 0.f, 0.f};
}
template <int A, int M, int B> DI void zero(Extra<A, M, B>& e) {
#pragma unroll
  for (int i = 0; i < (A ? A : 1); i++) e.cs[i] = f32x4{0.f, 0.f, 0.f, 0.f};
#pragma unroll
  for (int i = 0; i < (B ? B : 1); i++) e.ts[i] = e.tq[i] = f32x4{0.f, 0.f, 0.f, 0.f};
}
DI bf16x8 ones_frag() { u32x4 o = {0x3f803f80u, 0x3f803f80u, 0x3f803f80u, 0x3f803f80u}; return __builtin_bit_cast(bf16x8, o); }
#define CBAR() asm volatile("s_waitcnt lgkmcnt(0)\n\ts_barrier" ::: "memory")

extern __shared__ __attribute__((aligned(16))) char smem[];
#define T1 0
#define T2 1

#define ROT1 ((rt + ct * 3) % 10)
#define AUXF ((LAS float*)(lds + LDS_AUX))
DI f32x4 ldsf4(LAS const float* p) { return *(LAS const f32x4*)p; }
DI float sel4(f32x4 v, int k) { float a = (k & 1) ? v[1] : v[0], b = (k & 1) ? v[3] : v[2]; return (k & 2) ? b : a; }

__global__ __launch_bounds__(512) void phase1(const char* __restrict__ x, const char* __restrict__ wqkv, const char* __restrict__ w1, char* __restrict__ ws,
                                              const char* __restrict__ wo, const char* __restrict__ w2, const char* __restrict__ w3, const char* __restrict__ w4) {
  typedef CfgA1 C;
  const int b = blockIdx.x, xcd = b & 7, idx = b >> 3;
  const int rt = (xcd >> 1) * 4 + (idx & 3), ct = (xcd & 1) * 8 + (idx >> 2);
  if (ct >= 15) return;
  const int wid = __builtin_amdgcn_readfirstlane(threadIdx.x >> 6), lane = threadIdx.x & 63;
  LAS char* lds = (LAS char*)smem;
  STAMP(0, 0);
  if (wid >= 4) {
    const int w = wid - 4;
    unsigned tv[C::PT], wv[C::PW];
    const char* wb = ct < 10 ? wqkv + (640 + ct * 128) * 2 : w1 + (ct - 10) * 256;
    const int pitch = ct < 10 ? 3840 : 1280;
    make_tokv<C>(tv, w, lane, 0); make_wv<C>(wv, w, lane, pitch);
    const unsigned gid = min(b * 4 + w, 719);
    const char* p1 = gid < 320 ? x + gid * 8192 : (gid < 620 ? wqkv + (gid - 320) * 8192 : w1 + (gid - 620) * 8192);
    loader<C, T1>(w, (unsigned)(size_t)lds, x + (size_t)rt * 128 * 1280, 128, tv, wb, 64 * pitch, wv, 10, ROT1, p1, lane * 128);
    STAMP(0, 1);
    return;
  }
  const int wm = wid >> 1, wn = wid & 1, g = lane >> 4, tk = lane & 15;
  CAddr<C> a; make_caddr<C>(a, wm, wn, lane, 2 * wn, 2 * wm);
  f32x4 acc[4][4]; zero(acc);
  Extra<2, 0, 2> ex; zero(ex);
  const bf16x8 ones = ones_frag();
  Frags<C, 4> fa;
  CBAR(); STAMP(0, 1); CSTAMP(0, 6); load_all<C, 4>(fa, lds, a, 0);
  if (ct < 10) consume<C, 4, false, true>(acc, ex, fa, lds, a, 0, 10, ones);
  else consume<C, 4, true, true>(acc, ex, fa, lds, a, 0, 10, ones);
  STAMP(0, 2); CSTAMP(0, 7);
  if (T2) {
    const unsigned gid = b * 4 + wid;
    const char* p2 = gid < 100 ? wo + gid * 8192 : (gid < 200 ? w2 + (gid - 100) * 8192 : (gid < 440 ? w3 + (gid - 200) * 8192 : w4 + (gid - 440) * 8192));
    if (gid < 680) dma4(p2, lane * 128, (unsigned)(size_t)lds + LDS_AUX + 3584);
  }
  {
#pragma unroll
    for (int i = 0; i < 2; i++) {
      float m = ex.ts[i][0] * (1.0f / D_), q = sel4(ex.tq[i], tk & 3);
      float var = fmaxf(q * (1.0f / D_) - m * m, 0.f);
      float rs = __builtin_amdgcn_rsqf(var + EPS);
      float r = rs;
      if (ct >= 10) r = rs * __builtin_amdgcn_rsqf(var * rs * rs + EPS);
      int row = wm * 64 + (2 * wn + i) * 16 + tk;
      if (g == (tk >> 2)) {
        AUXF[row] = r; AUXF[128 + row] = r * m;
        if (ct == 0) ((f32x2*)(ws + OFF_ST))[rt * 128 + row] = f32x2{m, rs};
      }
      if (g == 0) AUXF[256 + wn * 64 + (2 * wm + i) * 16 + tk] = ex.cs[i][0];
    }
  }
  CBAR();
  STAMP(0, 3);
  if (ct < 10) {
    char* dst = ws + (ct < 5 ? OFF_PK : OFF_V) + lane * 16;
    const int cb0 = (ct < 5 ? ct : ct - 5) * 8 + wn * 4;
    float cs[4]; f32x2 zs[4];
#pragma unroll
    for (int j = 0; j < 4; j++) { cs[j] = AUXF[256 + wn * 64 + ((j + 2 * wm) & 3) * 16 + tk]; zs[j] = f32x2{0.f, 0.f}; }
#pragma unroll
    for (int ip = 0; ip < 2; ip++) {
      const int tb = wm * 64 + (ip ^ wn) * 32 + 4 * g;
      f32x4 r0 = ldsf4(AUXF + tb), r1 = ldsf4(AUXF + tb + 16), m0 = ldsf4(AUXF + 128 + tb), m1 = ldsf4(AUXF + 128 + tb + 16);
      char* d2 = dst + (size_t)((rt * 4 + wm * 2 + (ip ^ wn)) * 40 + cb0) * 1024;
#pragma unroll
      for (int j = 0; j < 4; j++) {
        f32x4 t0 = r0 * acc[2 * ip][j] - m0 * cs[j], t1 = r1 * acc[2 * ip + 1][j] - m1 * cs[j];
        u32x4 o;
        if (ct < 5) {
          o = u32x4{pk2v(phi2(lo2(t0))), pk2v(phi2(hi2(t0))), pk2v(phi2(lo2(t1))), pk2v(phi2(hi2(t1)))};
          zs[j] += (unpk(o[0]) + unpk(o[1])) + (unpk(o[2]) + unpk(o[3]));
        } else {
          o = u32x4{pk2v(lo2(t0)), pk2v(hi2(t0)), pk2v(lo2(t1)), pk2v(hi2(t1))};
        }
        st16(d2 + ((j + 2 * wm) & 3) * 1024, o);
      }
    }
    if (ct < 5) {
      float z[4];
#pragma unroll
      for (int j = 0; j < 4; j++) { z[j] = zs[j][0] + zs[j][1]; z[j] += shx(z[j], 16); z[j] += shx(z[j], 32); }
      if (wm == 1 && lane < 16) {
#pragma unroll
        for (int j = 0; j < 4; j++) AUXF[704 + wn * 64 + ((j + 2) & 3) * 16 + lane] = z[j];
      }
      CBAR();
      if (wm == 0 && lane < 16) {
        float* zp = (float*)(ws + OFF_ZP) + rt * 640 + ct * 128 + wn * 64;
#pragma unroll
        for (int j = 0; j < 4; j++) zp[j * 16 + lane] = z[j] + AUXF[704 + wn * 64 + j * 16 + lane];
      }
    }
  } else {
    const int c0 = (ct - 10) * 128 + wn * 64;
#pragma unroll
    for (int i = 0; i < 4; i++) {
      const int row = wm * 64 + ((i + 2 * wn) & 3) * 16 + tk;
      const float rr = AUXF[row], rm = AUXF[128 + row];
#pragma unroll
      for (int jp = 0; jp < 2; jp++) {
        const int jt = jp ^ wm;
        u32x4 o;
#pragma unroll
        for (int h = 0; h < 2; h++) {
          f32x4 cs = ldsf4(AUXF + 256 + wn * 64 + (2 * jt + h) * 16 + 4 * g);
          f32x4 t = rr * acc[i][2 * jp + h] - rm * cs;
          o[2 * h] = pk2v(gelu2(lo2(t))); o[2 * h + 1] = pk2v(gelu2(hi2(t)));
        }
        const int col = c0 + jt * 32, grp = col / 160;
        const int dl = grp == 0 ? 1 : (grp == 1 ? -1 : (grp == 2 ? 2 : -2));
        int td = rt * 128 + row + dl;
        const bool ok = td >= 0 && td < S_;
        td &= S_ - 1;
        if (!ok) o = u32x4{0, 0, 0, 0};
        st16(ws + OFF_H + ((size_t)((td >> 4) * 20 + (col >> 5)) * 64 + g * 16 + (td & 15)) * 16, o);
      }
    }
  }
  waitvm<0>();
  STAMPEND(0);
}

__global__ __launch_bounds__(512) void phase2(const char* __restrict__ x, const char* __restrict__ wqkv, const char* __restrict__ w2, char* __restrict__ ws) {
  const int b = blockIdx.x;
  const int wid = __builtin_amdgcn_readfirstlane(threadIdx.x >> 6), lane = threadIdx.x & 63;
  LAS char* lds = (LAS char*)smem;
  const int g = lane >> 4, tk = lane & 15;
  STAMP(1, 0);
  if (b < 128) {
    typedef CfgQ C;
    const int rt = b >> 2, ct = b & 3;
    if (wid >= 4) {
      const int w = wid - 4;
      unsigned tv[C::PT], wv[C::PW];
      make_tokv<C>(tv, w, lane, 0); make_wv<C>(wv, w, lane, 3840);
      loader<C>(w, (unsigned)(size_t)lds, x + (size_t)rt * 64 * 1280, 128, tv, wqkv + ct * 320, 64 * 3840, wv, 10);
      STAMP(1, 1);
      return;
    }
    const int wm = wid >> 1, wn = wid & 1;
    float zp[16];
    const int t = threadIdx.x;
    if (t < 160) {
      const float* p = (const float*)(ws + OFF_ZP) + ct * 160 + t;
#pragma unroll
      for (int r = 0; r < 16; r++) zp[r] = p[r * 640];
    }
    f32x2 st[2];
#pragma unroll
    for (int i = 0; i < 2; i++) st[i] = ((const f32x2*)(ws + OFF_ST))[rt * 64 + wm * 32 + i * 16 + tk];
    CAddr<C> a; make_caddr<C>(a, wm, wn, lane);
    f32x4 acc[2][5]; zero(acc);
    Extra<5, 1, 0> ex; zero(ex);
    const bf16x8 ones = ones_frag();
    Frags<C, 5> fa;
    CBAR(); STAMP(1, 1); load_all<C, 5>(fa, lds, a, 0);
    if (t < 160) {
      float z = 0.f;
#pragma unroll
      for (int r = 0; r < 16; r += 4) z += (zp[r] + zp[r + 1]) + (zp[r + 2] + zp[r + 3]);
      AUXF[512 + t] = z;
    }
    consume<C, 5, true, true>(acc, ex, fa, lds, a, 0, 10, ones);
    STAMP(1, 2);
#pragma unroll
    for (int i = 0; i < 2; i++) {
      const float rs = st[i][1], rm = rs * st[i][0];
      f32x4 pq[5]; float part[5];
#pragma unroll
      for (int j = 0; j < 5; j++) {
        f32x4 z = ldsf4(AUXF + 512 + wn * 80 + j * 16 + 4 * g);
        f32x4 tt = rs * acc[i][j] - rm * ex.cs[j];
        f32x2 p0 = phi2(lo2(tt)), p1 = phi2(hi2(tt));
        pq[j] = f32x4{p0[0], p0[1], p1[0], p1[1]};
        f32x2 s = p0 * lo2(z) + p1 * hi2(z);
        part[j] = s[0] + s[1];
      }
      float dh[4];
      dh[0] = part[0] + (g == 0 ? part[1] : 0.f);
      dh[1] = (g >= 1 ? part[1] : 0.f) + (g <= 1 ? part[2] : 0.f);
      dh[2] = (g >= 2 ? part[2] : 0.f) + (g <= 2 ? part[3] : 0.f);
      dh[3] = (g == 3 ? part[3] : 0.f) + part[4];
#pragma unroll
      for (int h = 0; h < 4; h++) { dh[h] += shx(dh[h], 16); dh[h] += shx(dh[h], 32); dh[h] = rcp(dh[h] + 1e-6f); }
      const float sc[5] = {dh[0], g == 0 ? dh[0] : dh[1], g <= 1 ? dh[1] : dh[2], g <= 2 ? dh[2] : dh[3], dh[3]};
#pragma unroll
      for (int j = 0; j < 5; j++) pq[j] *= sc[j];
      char* dst = ws + OFF_HP + ((size_t)(rt * 4 + wm * 2 + i) * 68 + 48 + ct * 5) * 1024 + lane * 16;
      st16(dst + (3 * wn) * 1024, u32x4{pk2v(lo2(pq[0])), pk2v(hi2(pq[0])), pk2v(lo2(pq[1])), pk2v(hi2(pq[1]))});
      st16(dst + (3 * wn + 1) * 1024, u32x4{pk2v(lo2(pq[2])), pk2v(hi2(pq[2])), pk2v(lo2(pq[3])), pk2v(hi2(pq[3]))});
      *(u32x2*)(dst + 2 * 1024 + wn * 8) = u32x2{pk2v(lo2(pq[4])), pk2v(hi2(pq[4]))};
    }
    STAMPEND(1);
  } else if (b < 208) {
    typedef CfgA C;
    const int bb = b - 128, rt = (bb & 7) * 2 + (bb >> 3) / 5, ct = (bb >> 3) % 5;
    if (wid >= 4) {
      const int w = wid - 4;
      unsigned tv[C::PT], wv[C::PW];
      make_tokv<C>(tv, w, lane, 20); make_wv<C>(wv, w, lane, 1280);
      loader<C>(w, (unsigned)(size_t)lds, ws + OFF_H + (size_t)rt * 8 * 20 * 1024, 2048, tv, w2 + ct * 256, 64 * 1280, wv, 10);
      STAMP(1, 1);
      return;
    }
    const int wm = wid >> 1, wn = wid & 1;
    u32x2 xv[4][4]; f32x2 st[4];
    CAddr<C> a; make_caddr<C>(a, wm, wn, lane);
    f32x4 acc[4][4]; zero(acc);
    Extra<0, 0, 0> ex;
    const bf16x8 ones = ones_frag();
    Frags<C, 4> fa;
    CBAR(); STAMP(1, 1); load_all<C, 4>(fa, lds, a, 0);
    consume<C, 4, true, false>(acc, ex, fa, lds, a, 0, 7, ones);
#pragma unroll
    for (int i = 0; i < 4; i++) {
      int row = rt * 128 + wm * 64 + i * 16 + tk;
      st[i] = ((const f32x2*)(ws + OFF_ST))[row];
#pragma unroll
      for (int j = 0; j < 4; j++) xv[i][j] = *(const u32x2*)(x + (size_t)row * 1280 + (ct * 128 + wn * 64 + j * 16 + 4 * g) * 2);
    }
    consume<C, 4, true, true>(acc, ex, fa, lds, a, 7, 10, ones);
    STAMP(1, 2);
#pragma unroll
    for (int i = 0; i < 4; i++) {
      const float rs = st[i][1], rm = st[i][0] * rs;
      f32x2 s1 = {0.f, 0.f}, s2 = {0.f, 0.f};
      char* dst = ws + OFF_X1 + ((size_t)(rt * 8 + wm * 4 + i) * 20 + ct * 4 + wn * 2) * 1024 + lane * 16;
#pragma unroll
      for (int jp = 0; jp < 2; jp++) {
        u32x4 o;
#pragma unroll
        for (int h = 0; h < 2; h++) {
          u32x2 xx = xv[i][2 * jp + h]; f32x4 c = acc[i][2 * jp + h] - rm;
          o[2 * h] = pk2v(unpk(xx[0]) * rs + lo2(c));
          o[2 * h + 1] = pk2v(unpk(xx[1]) * rs + hi2(c));
        }
#pragma unroll
        for (int e = 0; e < 4; e++) { f32x2 v = unpk(o[e]); s1 += v; s2 += v * v; }
        st16(dst + jp * 1024, o);
      }
      float a1 = s1[0] + s1[1], a2 = s2[0] + s2[1];
      a1 += shx(a1, 16); a2 += shx(a2, 16); a1 += shx(a1, 32); a2 += shx(a2, 32);
      if (g == 0) ((f32x2*)(ws + OFF_XS))[(size_t)(rt * 128 + wm * 64 + i * 16 + tk) * 10 + ct * 2 + wn] = f32x2{a1, a2};
    }
    STAMPEND(1);
  } else if (b < 240) {
    const int bb = b - 208, G = bb & 7, R = bb >> 3;
    f32x4 acc[13];
#pragma unroll
    for (int i = 0; i < 13; i++) acc[i] = f32x4{0.f, 0.f, 0.f, 0.f};
    bf16x8 pk[2][5], vv[2][5];
#pragma unroll
    for (int u = 0; u < 2; u++)
#pragma unroll
      for (int j = 0; j < 5; j++) {
        size_t o = ((size_t)(R * 16 + wid * 2 + u) * 40 + G * 5 + j) * 1024 + lane * 16;
        pk[u][j] = *(const bf16x8*)(ws + OFF_PK + o); vv[u][j] = *(const bf16x8*)(ws + OFF_V + o);
      }
#pragma unroll
    for (int u = 0; u < 2; u++)
#pragma unroll
      for (int db = 0; db < 5; db++)
#pragma unroll
        for (int eb = db - 1; eb <= db + 1; eb++)
          if (eb >= 0 && eb < 5) acc[2 * db + eb] = mfma(pk[u][db], vv[u][eb], acc[2 * db + eb]);
    STAMP(1, 2);
#pragma unroll
    for (int i = 0; i < 13; i++) *(LAS f32x4*)(lds + (wid * 13 + i) * 1024 + lane * 16) = acc[i];
    __syncthreads();
    for (int o = threadIdx.x; o < 832; o += 512) {
      f32x4 s = *(LAS const f32x4*)(lds + o * 16);
#pragma unroll
      for (int w = 1; w < 8; w++) s += *(LAS const f32x4*)(lds + w * 13 * 1024 + o * 16);
      *(f32x4*)(ws + OFF_KVP + (size_t)(R * 8 + G) * 13 * 1024 + o * 16) = s;
    }
    STAMPEND(1);
  }
}

__global__ __launch_bounds__(512) void phase3(const char* __restrict__ wo, const char* __restrict__ w3, const char* __restrict__ w4, char* __restrict__ ws) {
  const int b = blockIdx.x;
  const int wid = __builtin_amdgcn_readfirstlane(threadIdx.x >> 6), lane = threadIdx.x & 63;
  LAS char* lds = (LAS char*)smem;
  STAMP(2, 0);
  if (b < 192) {
    typedef CfgA C;
    const int xcd = b & 7, idx = b >> 3;
    const int rt = (xcd >> 1) * 4 + (idx & 3), ct = (xcd & 1) * 6 + (idx >> 2);
    if (wid >= 4) {
      const int w = wid - 4;
      unsigned tv[C::PT], wv[C::PW];
      make_tokv<C>(tv, w, lane, 20); make_wv<C>(wv, w, lane, 3072);
      loader<C>(w, (unsigned)(size_t)lds, ws + OFF_X1 + (size_t)rt * 8 * 20 * 1024, 2048, tv, w3 + ct * 256, 64 * 3072, wv, 10);
      STAMP(2, 1);
      return;
    }
    const int wm = wid >> 1, wn = wid & 1, g = lane >> 4, tk = lane & 15;
    f32x4 xs[5];
    if (wid < 2) {
      const f32x4* p = (const f32x4*)(ws + OFF_XS + (size_t)(rt * 128 + threadIdx.x) * 80);
#pragma unroll
      for (int i = 0; i < 5; i++) xs[i] = p[i];
    }
    CAddr<C> a; make_caddr<C>(a, wm, wn, lane);
    f32x4 acc[4][4]; zero(acc);
    Extra<4, 1, 0> ex; zero(ex);
    const bf16x8 ones = ones_frag();
    Frags<C, 4> fa;
    CBAR(); STAMP(2, 1); load_all<C, 4>(fa, lds, a, 0);
    if (wid < 2) {
      f32x4 s = (xs[0] + xs[1]) + (xs[2] + xs[3]) + xs[4];
      float m = (s[0] + s[2]) * (1.0f / D_), var = fmaxf((s[1] + s[3]) * (1.0f / D_) - m * m, 0.f);
      float rs = __builtin_amdgcn_rsqf(var + EPS);
      AUXF[threadIdx.x] = rs; AUXF[128 + threadIdx.x] = rs * m;
    }
    consume<C, 4, true, true>(acc, ex, fa, lds, a, 0, 10, ones);
    STAMP(2, 2);
#pragma unroll
    for (int i = 0; i < 4; i++) {
      const int row = wm * 64 + i * 16 + tk;
      const float rs = AUXF[row], rm = AUXF[128 + row];
      char* dst = ws + OFF_HP + ((size_t)(rt * 8 + wm * 4 + i) * 68 + ct * 4 + wn * 2) * 1024 + lane * 16;
#pragma unroll
      for (int jp = 0; jp < 2; jp++) {
        u32x4 o;
#pragma unroll
        for (int h = 0; h < 2; h++) {
          f32x4 t = rs * acc[i][2 * jp + h] - rm * ex.cs[2 * jp + h];
          o[2 * h] = pk2v(gelu2(lo2(t))); o[2 * h + 1] = pk2v(gelu2(hi2(t)));
        }
        st16(dst + jp * 1024, o);
      }
    }
    STAMPEND(2);
  } else {
    const int bb = b - 192, h = bb >> 1, half = bb & 1, t = threadIdx.x;
    LAS float* kv = (LAS float*)lds;
    LAS char* wt = lds + 2048;
    u32x4 wl[4];
#pragma unroll
    for (int i = 0; i < 4; i++) {
      int s = i * 512 + t;
      if (s < 1920) wl[i] = *(const u32x4*)(w4 + (size_t)(96 * (bb >> 2) + s / 20) * 1280 + (bb & 3) * 320 + (s % 20) * 16);
    }
    float kvv = 0.f;
    if (t < 400) {
      int d = t % 20, e = t / 20;
      int hh = h & 3, G = h >> 2, da = 20 * hh + d, ea = 20 * hh + e;
      int fi = (2 * (da >> 4) + (ea >> 4)) * 256 + (((da & 15) >> 2) * 16 + (ea & 15)) * 4 + (da & 3);
      const float* p = (const float*)(ws + OFF_KVP) + G * 13 * 256 + fi;
      kvv = (p[0] + p[8 * 13 * 256]) + (p[2 * 8 * 13 * 256] + p[3 * 8 * 13 * 256]);
    }
    const int cp = t % 160, dg = t / 160;
    unsigned wv[20];
    if (dg < 3) {
#pragma unroll
      for (int e = 0; e < 20; e++) wv[e] = *(const unsigned*)(wo + (size_t)(20 * h + e) * 1280 + (half * 320 + cp * 2) * 2);
    }
    if (t < 400) kv[t] = kvv;
#pragma unroll
    for (int i = 0; i < 4; i++) {
      int s = i * 512 + t;
      if (s < 1920) *(LAS u32x4*)(wt + s * 16) = wl[i];
    }
    __syncthreads();
    {
      const int gq = lane >> 4, q = (lane >> 2) & 3, p = lane & 3;
      for (int f = wid; f < 30; f += 8) {
        int ks = f / 10, nb = f % 10;
        LAS const char* src = wt + (ks * 32 + 4 * gq + q) * 320 + nb * 32 + p * 8;
        bf16x8 v = cat(ldstr(src), ldstr(src + 16 * 320));
        st16(ws + OFF_WT + ((size_t)(3 * (bb >> 2) + ks) * 40 + (bb & 3) * 10 + nb) * 1024 + lane * 16, __builtin_bit_cast(u32x4, v));
      }
    }
    if (dg < 3) {
      f32x4 a0[2], a1[2];
#pragma unroll
      for (int u = 0; u < 2; u++) a0[u] = a1[u] = f32x4{0.f, 0.f, 0.f, 0.f};
#pragma unroll
      for (int e = 0; e < 20; e++) {
        const float wl_ = bflo(wv[e]), wh_ = bfhi(wv[e]);
#pragma unroll
        for (int u = 0; u < 2; u++) { f32x4 k = ldsf4(kv + e * 20 + 8 * dg + 4 * u); a0[u] += k * wl_; a1[u] += k * wh_; }
      }
#pragma unroll
      for (int u = 0; u < 2; u++) {
        const int q = dg * 2 + u;
        if (q < 5) {
          int c = 20 * h + 4 * q, beta = c >> 4, G = beta / 10, j = beta % 10, wn = j / 5, jj = j % 5;
          int kap = jj == 4 ? 5 * G + 2 : 5 * G + 3 * wn + (jj >> 1), tt = jj == 4 ? wn : (jj & 1);
          int n = half * 320 + cp * 2;
          char* dst = ws + OFF_WT + ((size_t)(48 + kap) * 40 + (n >> 4)) * 1024 + (((c & 15) >> 2) * 16 + (n & 15)) * 16 + tt * 8;
          *(u32x2*)dst = u32x2{pk2v(lo2(a0[u])), pk2v(hi2(a0[u]))};
          *(u32x2*)(dst + 16) = u32x2{pk2v(lo2(a1[u])), pk2v(hi2(a1[u]))};
        }
      }
    }
    STAMPEND(2);
  }
}

template <int NBW>
DI void p4_consumer(const char* __restrict__ x, char* __restrict__ out, char* __restrict__ ws, LAS char* lds, int rt, int ct, int wm, int wn, int lane) {
  typedef CfgC C;
  const int g = lane >> 4, tk = lane & 15;
  u32x2 xv[2][NBW], x1v[2][NBW];
  CAddr<C> a; make_caddr<C>(a, wm, wn, lane);
  f32x4 h2[2][NBW], sa[2][NBW]; zero(h2); zero(sa);
  Extra<0, 0, 0> ex;
  const bf16x8 ones = ones_frag();
  Frags<C, NBW> fa;
  CBAR(); STAMP(3, 1); load_all<C, NBW>(fa, lds, a, 0);
  consume<C, NBW, true, false>(h2, ex, fa, lds, a, 0, 12, ones);
#pragma unroll
  for (int i = 0; i < 2; i++) {
    int row = rt * 64 + wm * 32 + i * 16 + tk;
#pragma unroll
    for (int j = 0; j < NBW; j++) {
      int blk = ct * 5 + wn * 3 + j;
      xv[i][j] = *(const u32x2*)(x + (size_t)row * 1280 + (blk * 16 + 4 * g) * 2);
      x1v[i][j] = *(const u32x2*)(ws + OFF_X1 + ((size_t)(row >> 4) * 20 + (blk >> 1)) * 1024 + lane * 16 + (blk & 1) * 8);
    }
  }
  consume<C, NBW, true, true>(sa, ex, fa, lds, a, 12, 17, ones);
  STAMP(3, 2);
#pragma unroll
  for (int i = 0; i < 2; i++) {
    int row = rt * 64 + wm * 32 + i * 16 + tk;
#pragma unroll
    for (int j = 0; j < NBW; j++) {
      int blk = ct * 5 + wn * 3 + j;
      u32x2 o;
#pragma unroll
      for (int e = 0; e < 2; e++) {
        f32x2 bb = unpk(x1v[i][j][e]) + (e ? hi2(h2[i][j]) : lo2(h2[i][j]));
        f32x2 z = bb * -1.4426950408889634f;
        f32x2 d = f32x2{ex2(z[0]), ex2(z[1])} + 1.0f;
        f32x2 sl = bb * f32x2{rcp(d[0]), rcp(d[1])};
        o[e] = pk2v(unpk(xv[i][j][e]) + (e ? hi2(sa[i][j]) : lo2(sa[i][j])) * sl);
      }
      *(u32x2*)(out + (size_t)row * 1280 + (blk * 16 + 4 * g) * 2) = o;
    }
  }
  STAMPEND(3);
}
__global__ __launch_bounds__(512) void phase4(const char* __restrict__ x, char* __restrict__ ws, char* __restrict__ out) {
  typedef CfgC C;
  const int b = blockIdx.x, xcd = b & 7, idx = b >> 3;
  const int rt = (xcd >> 1) * 8 + (idx & 7), ct = (xcd & 1) * 4 + (idx >> 3);
  const int wid = __builtin_amdgcn_readfirstlane(threadIdx.x >> 6), lane = threadIdx.x & 63;
  LAS char* lds = (LAS char*)smem;
  STAMP(3, 0);
  if (wid >= 4) {
    const int w = wid - 4;
    unsigned tv[C::PT], wv[C::PW];
    make_tokv<C>(tv, w, lane, 68); make_wv<C>(wv, w, lane, 0);
    loader<C>(w, (unsigned)(size_t)lds, ws + OFF_HP + (size_t)rt * 4 * 68 * 1024, 4096, tv, ws + OFF_WT + ct * 5 * 1024, 4 * 40 * 1024, wv, 17);
    STAMP(3, 1);
    return;
  }
  const int wm = wid >> 1, wn = wid & 1;
  if (wn == 0) p4_consumer<3>(x, out, ws, lds, rt, ct, wm, wn, lane);
  else p4_consumer<2>(x, out, ws, lds, rt, ct, wm, wn, lane);
}

extern "C" void kernel_launch(void* const* d_in, const int* in_sizes, int n_in, void* d_out, int out_size, void* d_ws, size_t ws_size, hipStream_t stream) {
  const char* x = (const char*)d_in[0]; const char* wqkv = (const char*)d_in[1]; const char* wo = (const char*)d_in[2];
  const char* w1 = (const char*)d_in[4]; const char* w2 = (const char*)d_in[5]; const char* w3 = (const char*)d_in[6]; const char* w4 = (const char*)d_in[7];
  char* ws = (char*)d_ws;
  (void)hipFuncSetAttribute((const void*)phase1, hipFuncAttributeMaxDynamicSharedMemorySize, LDS_TOTAL);
  (void)hipFuncSetAttribute((const void*)phase2, hipFuncAttributeMaxDynamicSharedMemorySize, LDS_TOTAL);
  (void)hipFuncSetAttribute((const void*)phase3, hipFuncAttributeMaxDynamicSharedMemorySize, LDS_TOTAL);
  (void)hipFuncSetAttribute((const void*)phase4, hipFuncAttributeMaxDynamicSharedMemorySize, LDS_TOTAL);
  phase1<<<256, 512, LDS_TOTAL, stream>>>(x, wqkv, w1, ws, wo, w2, w3, w4);
  phase2<<<240, 512, LDS_TOTAL, stream>>>(x, wqkv, w2, ws);
  phase3<<<256, 512, LDS_TOTAL, stream>>>(wo, w3, w4, ws);
  phase4<<<256, 512, LDS_TOTAL, stream>>>(x, ws, (char*)d_out);
}
```

```cpp
#include <hip/hip_runtime.h>
#include <stdint.h>

typedef __attribute__((ext_vector_type(8))) __bf16 bf16x8;
typedef __attribute__((ext_vector_type(2))) __bf16 bf16x2;
typedef __attribute__((ext_vector_type(4))) float f32x4;
typedef __attribute__((ext_vector_type(2))) float f32x2;
typedef __attribute__((ext_vector_type(4))) short v4i16;
typedef __attribute__((ext_vector_type(8))) short v8i16;
typedef __attribute__((ext_vector_type(4))) unsigned u32x4;
typedef __attribute__((ext_vector_type(2))) unsigned u32x2;
#define LAS __attribute__((address_space(3)))
#define DI __device__ __forceinline__
#define SB() __builtin_amdgcn_sched_barrier(0)

constexpr int S_ = 2048, D_ = 640, F_ = 1536;
constexpr size_t KB = 1024;
constexpr size_t OFF_PK = 0;
constexpr size_t OFF_V = OFF_PK + 64 * 40 * KB;
constexpr size_t OFF_H = OFF_V + 64 * 40 * KB;
constexpr size_t OFF_X1 = OFF_H + 128 * 20 * KB;
constexpr size_t OFF_H3 = OFF_X1 + 128 * 20 * KB;
constexpr size_t OFF_PQ = OFF_H3 + 128 * 48 * KB;
constexpr size_t OFF_SA = OFF_PQ + 128 * 20 * KB;
constexpr size_t OFF_WQ = OFF_SA + 128 * 20 * KB;
constexpr size_t OFF_W2 = OFF_WQ + 20 * 40 * KB;
constexpr size_t OFF_W3 = OFF_W2 + 20 * 40 * KB;
constexpr size_t OFF_W4 = OFF_W3 + 20 * 96 * KB;
constexpr size_t OFF_ME = OFF_W4 + 48 * 40 * KB;
constexpr size_t OFF_CSQ = OFF_ME + 20 * 40 * KB;
constexpr size_t OFF_CS3 = OFF_CSQ + 10 * 640 * 4;
constexpr size_t OFF_ZP = OFF_CS3 + 10 * 1536 * 4;
constexpr size_t OFF_ST = OFF_ZP + 16 * 640 * 4;
constexpr size_t OFF_XS = OFF_ST + 2048 * 8;
constexpr size_t OFF_KVP = OFF_XS + 2048 * 80;
constexpr size_t OFF_FLAG = OFF_KVP + 4 * 8 * 13 * KB;
constexpr float EPS = 1e-5f;
#define STAMP(k, i)
#define STAMPEND(k)
#define CSTAMP(k, i)

DI void dma16(const void* sbase, unsigned voff, unsigned ldsdst) {
  unsigned keep;
  asm volatile("s_mov_b32 %0, m0\n\ts_mov_b32 m0, %3\n\ts_nop 0\n\tglobal_load_lds_dwordx4 %1, %2\n\ts_mov_b32 m0, %0"
               : "=&s"(keep) : "v"(voff), "s"(sbase), "s"(ldsdst) : "memory");
}
DI void dma4(const void* sbase, unsigned voff, unsigned ldsdst) {
  unsigned keep;
  asm volatile("s_mov_b32 %0, m0\n\ts_mov_b32 m0, %3\n\ts_nop 0\n\tglobal_load_lds_dword %1, %2\n\ts_mov_b32 m0, %0"
               : "=&s"(keep) : "v"(voff), "s"(sbase), "s"(ldsdst) : "memory");
}
DI void st16(void* p, u32x4 v) { *(u32x4*)p = v; }
DI void st16wt(void* p, u32x4 v) { asm volatile("global_store_dwordx4 %0, %1, off sc1\n\ts_nop 1" ::"v"(p), "v"(v) : "memory"); }
DI float dot2(unsigned a, unsigned b, float c) { return __builtin_amdgcn_fdot2_f32_bf16(__builtin_bit_cast(bf16x2, a), __builtin_bit_cast(bf16x2, b), c, false); }
#define BAR() asm volatile("s_waitcnt lgkmcnt(0)\n\ts_barrier" ::: "memory")
template <int N> DI void waitvm() { asm volatile("s_waitcnt vmcnt(%0)" ::"n"(N) : "memory"); }
DI unsigned pk2(float lo, float hi) { f32x2 v = {lo, hi}; bf16x2 b = __builtin_convertvector(v, bf16x2); return __builtin_bit_cast(unsigned, b); }
DI unsigned pk2v(f32x2 v) { bf16x2 b = __builtin_convertvector(v, bf16x2); return __builtin_bit_cast(unsigned, b); }
DI float bflo(unsigned u) { return __uint_as_float(u << 16); }
DI float bfhi(unsigned u) { return __uint_as_float(u & 0xffff0000u); }
DI f32x2 unpk(unsigned u) { return f32x2{bflo(u), bfhi(u)}; }
DI float ex2(float x) { return __builtin_amdgcn_exp2f(x); }
DI float rcp(float x) { return __builtin_amdgcn_rcpf(x); }
DI f32x2 lo2(f32x4 v) { return f32x2{v[0], v[1]}; }
DI f32x2 hi2(f32x4 v) { return f32x2{v[2], v[3]}; }
DI f32x2 gelu2(f32x2 t) {
  const float c1 = -1.5957691216057308f * 1.4426950408889634f, c2 = c1 * 0.044715f;
  f32x2 u = t * t;
  f32x2 z = t * (u * c2 + c1);
  f32x2 d = f32x2{ex2(z[0]), ex2(z[1])} + 1.0f;
  return t * f32x2{rcp(d[0]), rcp(d[1])};
}
DI f32x2 phi2(f32x2 t) {
  f32x2 a = t + (1.0f + 1e-6f), z = t * 1.4426950408889634f;
  f32x2 e = f32x2{ex2(z[0]), ex2(z[1])} + 1e-6f;
  return f32x2{t[0] > 0.f ? a[0] : e[0], t[1] > 0.f ? a[1] : e[1]};
}
DI float shx(float v, int m) { return __shfl_xor(v, m, 64); }
DI bf16x8 lds128(LAS const char* p) { return *(LAS const bf16x8*)p; }
DI v4i16 ldstr(LAS const char* p) { return __builtin_amdgcn_ds_read_tr16_b64_v4i16((LAS v4i16*)p); }
DI bf16x8 cat(v4i16 a, v4i16 b) { v8i16 r = __builtin_shufflevector(a, b, 0, 1, 2, 3, 4, 5, 6, 7); return __builtin_bit_cast(bf16x8, r); }
DI f32x4 mfma(bf16x8 a, bf16x8 b, f32x4 c) { return __builtin_amdgcn_mfma_f32_16x16x32_bf16(a, b, c, 0, 0, 0); }

template <int MB_, int NB_, int NB0_, int KS_, int D_R, bool XROW_, bool FRAGW_>
struct Cfg {
  static constexpr int MB = MB_, NB = NB_, NB0 = NB0_, KS = KS_, D = D_R;
  static constexpr bool XROW = XROW_, FRAGW = FRAGW_;
  static constexpr int MBW = MB / 2, RB = NB * 32, TOKB = MB * KS * 1024, WB = KS * 32 * RB, STAGE = TOKB + WB;
  static constexpr int PT = TOKB / 4096, PW = WB / 4096, P = PT + PW;
};
typedef Cfg<8, 8, 4, 2, 4, true, false> CfgA1;
typedef Cfg<8, 8, 4, 2, 4, false, true> CfgA;
typedef Cfg<4, 10, 5, 2, 4, true, true> CfgQ;
typedef Cfg<8, 10, 5, 2, 4, false, true> CfgS;
typedef Cfg<4, 5, 3, 4, 4, false, true> CfgC;
constexpr int LDS_AUX = 147456;
constexpr int LDS_TOTAL = LDS_AUX + 4096;
#define RLX __ATOMIC_RELAXED, __HIP_MEMORY_SCOPE_AGENT

template <int RB> DI unsigned wswz(unsigned rho) { return RB == 256 ? 2 * (rho & 7) : (RB == 320 ? 2 * ((rho >> 2) & 1) : 0); }

template <class C, int XE, class Post, class Ext, class Fin>
DI void loader(int w, unsigned lds0, const char* tokbase, int tokstride, const unsigned (&tokv)[C::PT],
               const char* wb, int wstride, const unsigned (&wv)[C::PW], int NS, Post post, Ext ext, Fin fin) {
  static_assert(C::D == 4, "ring depth");
  auto issue = [&](int s) {
    const char* tb = tokbase + (size_t)s * tokstride;
    const char* wp = wb + (size_t)s * wstride;
    unsigned dst = lds0 + (s & 3) * C::STAGE + w * 1024;
#pragma unroll
    for (int i = 0; i < C::PT; i++) dma16(tb, tokv[i], dst + i * 4096);
#pragma unroll
    for (int i = 0; i < C::PW; i++) dma16(wp, wv[i], dst + C::TOKB + i * 4096);
  };
  issue(0); issue(1);
  waitvm<C::P>(); BAR();
  issue(2); issue(3); post(0);
  waitvm<2 * C::P>(); BAR();
  issue(4); post(1);
#pragma unroll 1
  for (int j = 2; j < NS - 3; j++) {
    waitvm<2 * C::P>(); BAR();
    issue(j + 3); post(j);
  }
  waitvm<2 * C::P>(); BAR();
  ext(lds0 + (NS & 3) * C::STAGE); post(NS - 3);
  waitvm<C::P + XE>(); BAR();
  post(NS - 2);
  waitvm<XE>(); BAR();
  post(NS - 1);
  fin();
}
struct Nop { DI void operator()() const {} DI void operator()(int) const {} DI void operator()(unsigned) const {} };

template <class C> DI void make_wv(unsigned (&wv)[C::PW], unsigned w, unsigned lane, unsigned pitch) {
#pragma unroll
  for (int i = 0; i < C::PW; i++) {
    if (C::FRAGW) {
      unsigned pc = i * 4 + w, ks = pc / C::NB, nb = pc % C::NB;
      wv[i] = (ks * pitch + nb) * 1024 + lane * 16;
    } else {
      unsigned sidx = (i * 4 + w) * 64 + lane;
      constexpr unsigned SL = C::RB / 16;
      unsigned rho = sidx / SL, c = sidx % SL;
      unsigned k = C::XROW ? ((rho & ~31u) | (((rho >> 2) & 3) << 3) | (((rho >> 4) & 1) << 2) | (rho & 3)) : rho;
      wv[i] = k * pitch + ((c ^ wswz<C::RB>(rho)) << 4);
    }
  }
}
template <class C> DI void make_tokv(unsigned (&tv)[C::PT], unsigned w, unsigned lane, unsigned kstot) {
#pragma unroll
  for (int i = 0; i < C::PT; i++) {
    unsigned qd = i * 4 + w;
    if (C::XROW) { unsigned row = qd * 8 + (lane >> 3); tv[i] = row * 1280 + (((lane & 7) ^ ((row >> 1) & 7)) << 4); }
    else { unsigned mb = qd / C::KS, ks = qd % C::KS; tv[i] = (mb * kstot + ks) * 1024 + lane * 16; }
  }
}

template <class C, int NBW> struct Frags { bf16x8 t[C::MBW]; v4i16 wl[NBW], wh[NBW]; bf16x8 wf[NBW]; };
template <class C> struct CAddr { int tok[2]; int tokb[2]; int wa[4]; };
template <class C> DI void make_caddr(CAddr<C>& a, int wm, int wn, int lane, int pi = 0, int pj = 0) {
  const int g = lane >> 4, q = (lane >> 2) & 3, p = lane & 3, t = lane & 15;
  if (C::XROW) {
    a.tok[0] = (wm * C::MBW * 16 + t) * 128 + ((g ^ ((t >> 1) & 7)) << 4);
    a.tok[1] = (wm * C::MBW * 16 + t) * 128 + (((4 + g) ^ ((t >> 1) & 7)) << 4);
  } else { a.tok[0] = wm * C::MBW * C::KS * 1024 + lane * 16; a.tok[1] = 0; }
  {
    const int rot = pi * (C::XROW ? 2048 : C::KS * 1024);
    a.tokb[0] = a.tok[0] - rot; a.tokb[1] = a.tok[1] - rot; a.tok[0] += rot; a.tok[1] += rot;
  }
  int r0 = 4 * g + q, base = C::TOKB + r0 * C::RB + (p >> 1) * 16 + (p & 1) * 8;
  if (C::FRAGW) { a.wa[0] = C::TOKB + wn * C::NB0 * 1024 + lane * 16; a.wa[1] = a.wa[2] = a.wa[3] = 0; }
  else if (C::RB == 256) {
#pragma unroll
    for (int j = 0; j < 4; j++) a.wa[j] = base + (((wn * 4 + ((j + pj) & 3)) ^ (r0 & 7)) << 5);
  } else {
    int b = g & 1, n0 = wn * C::NB0;
    a.wa[0] = base + ((n0 ^ b) << 5);
    a.wa[1] = base + (((n0 + 1) ^ b) << 5) - 32;
    a.wa[2] = a.wa[3] = 0;
  }
}
template <class C, int NBW> struct RD { static constexpr int WR = C::FRAGW ? 1 : 2, NR = C::MBW + WR * NBW; };
template <class C, int NBW>
DI void read_one(Frags<C, NBW>& f, LAS const char* slot, const CAddr<C>& a, int ks, int r) {
  constexpr int WR = RD<C, NBW>::WR;
  int ti = -1, wj = -1, h = 0;
  if (r == 0) ti = 0;
  else if (r <= WR) { wj = 0; h = r - 1; }
  else if (r < WR + C::MBW) ti = r - WR;
  else { int q = r - C::MBW; wj = q / WR; h = q % WR; }
  if (ti >= 0) {
    const bool hi = ti >= C::MBW / 2;
    f.t[ti] = C::XROW ? lds128(slot + (hi ? a.tokb[ks & 1] : a.tok[ks & 1]) + ti * 2048)
                      : lds128(slot + (hi ? a.tokb[0] : a.tok[0]) + (ti * C::KS + ks) * 1024);
  } else if (C::FRAGW) {
    f.wf[wj] = lds128(slot + a.wa[0] + (ks * C::NB + wj) * 1024);
  } else {
    LAS const char* pw = C::RB == 256 ? slot + a.wa[wj] : slot + a.wa[wj & 1] + wj * 32;
    if (h == 0) f.wl[wj] = ldstr(pw + ks * 32 * C::RB);
    else f.wh[wj] = ldstr(pw + (ks * 32 + 16) * C::RB);
  }
}
template <class C, int NBW> DI bf16x8 wfrag(const Frags<C, NBW>& f, int j) { return C::FRAGW ? f.wf[j] : cat(f.wl[j], f.wh[j]); }
template <class C, int NBW>
DI void load_all(Frags<C, NBW>& f, LAS const char* slot, const CAddr<C>& a, int ks) {
#pragma unroll
  for (int r = 0; r < RD<C, NBW>::NR; r++) read_one<C, NBW>(f, slot, a, ks, r);
}
template <int NCS_, int CSM_, int NTS_> struct Extra {
  static constexpr int NCS = NCS_, CSM = CSM_, NTS = NTS_;
  f32x4 cs[NCS_ ? NCS_ : 1], ts[NTS_ ? NTS_ : 1], tq[NTS_ ? NTS_ : 1];
};
template <class C, int NBW, bool SWAP, bool BARR, bool RDN, class E>
DI void kstep(f32x4 (&acc)[C::MBW][NBW], E& ex, const Frags<C, NBW>& cur, Frags<C, NBW>& nxt,
              LAS const char* nslot, const CAddr<C>& a, int nks, bf16x8 ones) {
  constexpr int MBW = C::MBW, NM = MBW * NBW, NR = RD<C, NBW>::NR;
  constexpr int F0 = BARR ? MBW : 0, NSL = NM - F0, PER = (NR + NSL - 1) / NSL;
#pragma unroll
  for (int m = 0; m < NM; m++) {
    const int j = m / MBW, i = m % MBW;
    bf16x8 wf = wfrag<C, NBW>(cur, j);
    acc[i][j] = SWAP ? mfma(wf, cur.t[i], acc[i][j]) : mfma(cur.t[i], wf, acc[i][j]);
    if (i == MBW - 1 && j < E::NCS) ex.cs[j] = E::CSM ? mfma(wf, ones, ex.cs[j]) : mfma(ones, wf, ex.cs[j]);
    if (j == 2 && i < E::NTS) ex.ts[i] = mfma(ones, cur.t[i], ex.ts[i]);
    if (j == 3 && i < E::NTS) ex.tq[i] = mfma(cur.t[i], cur.t[i], ex.tq[i]);
    if (BARR && m == F0 - 1) { SB(); BAR(); }
    if (RDN && m >= F0) {
#pragma unroll
      for (int r = (m - F0) * PER; r < (m - F0 + 1) * PER && r < NR; r++) read_one<C, NBW>(nxt, nslot, a, nks, r);
    }
    SB();
  }
}
template <class C, int NBW, bool SWAP, bool LAST, class E>
DI void consume(f32x4 (&acc)[C::MBW][NBW], E& ex, Frags<C, NBW>& fa, LAS const char* lds, const CAddr<C>& a, int s0, int s1, bf16x8 ones) {
  Frags<C, NBW> fb;
  const int e = LAST ? s1 - 1 : s1;
#pragma unroll 1
  for (int s = s0; s < e; s++) {
    LAS const char* slot = lds + (s & 3) * C::STAGE;
    LAS const char* nsl = lds + ((s + 1) & 3) * C::STAGE;
#pragma unroll
    for (int ks = 0; ks < C::KS; ks += 2) {
      kstep<C, NBW, SWAP, false, true>(acc, ex, fa, fb, slot, a, ks + 1, ones);
      if (ks + 2 < C::KS) kstep<C, NBW, SWAP, false, true>(acc, ex, fb, fa, slot, a, ks + 2, ones);
      else kstep<C, NBW, SWAP, true, true>(acc, ex, fb, fa, nsl, a, 0, ones);
    }
  }
  if (LAST) {
    LAS const char* slot = lds + (e & 3) * C::STAGE;
#pragma unroll
    for (int ks = 0; ks < C::KS; ks += 2) {
      kstep<C, NBW, SWAP, false, true>(acc, ex, fa, fb, slot, a, ks + 1, ones);
      if (ks + 2 < C::KS) kstep<C, NBW, SWAP, false, true>(acc, ex, fb, fa, slot, a, ks + 2, ones);
      else kstep<C, NBW, SWAP, false, false>(acc, ex, fb, fa, slot, a, 0, ones);
    }
  }
}
template <int A, int B> DI void zero(f32x4 (&acc)[A][B]) {
#pragma unroll
  for (int i = 0; i < A; i++)
#pragma unroll
    for (int j = 0; j < B; j++) acc[i][j] = f32x4{0.f, 0.f, 0.f, 0.f};
}
template <int A, int M, int B> DI void zero(Extra<A, M, B>& e) {
#pragma unroll
  for (int i = 0; i < (A ? A : 1); i++) e.cs[i] = f32x4{0.f, 0.f, 0.f, 0.f};
#pragma unroll
  for (int i = 0; i < (B ? B : 1); i++) e.ts[i] = e.tq[i] = f32x4{0.f, 0.f, 0.f, 0.f};
}
DI bf16x8 ones_frag() { u32x4 o = {0x3f803f80u, 0x3f803f80u, 0x3f803f80u, 0x3f803f80u}; return __builtin_bit_cast(bf16x8, o); }
#define CBAR() asm volatile("s_waitcnt lgkmcnt(0)\n\ts_barrier" ::: "memory")

extern __shared__ __attribute__((aligned(16))) char smem[];
#define AUXF ((LAS float*)(lds + LDS_AUX))
DI f32x4 ldsf4(LAS const float* p) { return *(LAS const f32x4*)p; }
DI float sel4(f32x4 v, int k) { float a = (k & 1) ? v[1] : v[0], b = (k & 1) ? v[3] : v[2]; return (k & 2) ? b : a; }

__global__ __launch_bounds__(512) void phase1(const char* __restrict__ x, const char* __restrict__ wqkv, const char* __restrict__ w1, char* __restrict__ ws,
                                              const char* __restrict__ wo, const char* __restrict__ w2, const char* __restrict__ w3, const char* __restrict__ w4) {
  typedef CfgA1 C;
  const int b = blockIdx.x, xcd = b & 7, idx = b >> 3;
  const int rt = (xcd >> 1) * 4 + (idx & 3), ct = (xcd & 1) * 8 + (idx >> 2);
  const int wid = __builtin_amdgcn_readfirstlane(threadIdx.x >> 6), lane = threadIdx.x & 63;
  LAS char* lds = (LAS char*)smem;
  STAMP(0, 0);
  if (ct >= 15) {
    LAS char* base = lds + wid * 16384;
    const unsigned lb = (unsigned)(size_t)base;
    const int g = lane >> 4, q = (lane >> 2) & 3, p = lane & 3, r0 = 4 * g + q;
    if (rt == 0 && wid == 0 && lane < 8) ((unsigned*)(ws + OFF_FLAG))[lane * 64] = 0u;
    if (rt * 8 + wid < 100) dma4(wo + (rt * 8 + wid) * 8192, lane * 128, (unsigned)(size_t)lds + LDS_AUX + 3584);
    const bf16x8 ones = ones_frag();
#pragma unroll 1
    for (int u = rt * 8 + wid; u < 340; u += 128) {
      const char* src; char* dst; float* cs = nullptr; int pitch, nbt; bool xr = false;
      if (u < 50) { int kb = u / 5, cb = u % 5; pitch = 3840; nbt = 40; xr = true; src = wqkv + kb * 64 * 3840 + cb * 256;
        dst = ws + OFF_WQ + (kb * 80 + cb * 8) * 1024; cs = (float*)(ws + OFF_CSQ) + kb * 640 + cb * 128; }
      else if (u < 100) { int v = u - 50, kb = v / 5, cb = v % 5; pitch = 1280; nbt = 40; src = w2 + kb * 64 * 1280 + cb * 256;
        dst = ws + OFF_W2 + (kb * 80 + cb * 8) * 1024; }
      else if (u < 220) { int v = u - 100, kb = v / 12, cb = v % 12; pitch = 3072; nbt = 96; src = w3 + kb * 64 * 3072 + cb * 256;
        dst = ws + OFF_W3 + (kb * 192 + cb * 8) * 1024; cs = (float*)(ws + OFF_CS3) + kb * 1536 + cb * 128; }
      else { int v = u - 220, kb = v / 5, cb = v % 5; pitch = 1280; nbt = 40; src = w4 + kb * 64 * 1280 + cb * 256;
        dst = ws + OFF_W4 + (kb * 80 + cb * 8) * 1024; }
#pragma unroll
      for (int i = 0; i < 16; i++) {
        const unsigned rho = i * 4 + g;
        const unsigned k = xr ? ((rho & ~31u) | (((rho >> 2) & 3) << 3) | (((rho >> 4) & 1) << 2) | (rho & 3)) : rho;
        dma16(src, k * pitch + (((lane & 15) ^ wswz<256>(rho)) << 4), lb + i * 1024);
      }
      waitvm<0>();
      LAS const char* pw = base + r0 * 256 + (p >> 1) * 16 + (p & 1) * 8;
#pragma unroll
      for (int nb = 0; nb < 8; nb++) {
        f32x4 c = {0.f, 0.f, 0.f, 0.f};
#pragma unroll
        for (int ks = 0; ks < 2; ks++) {
          LAS const char* pp = pw + ((nb ^ (r0 & 7)) << 5) + ks * 32 * 256;
          bf16x8 f = cat(ldstr(pp), ldstr(pp + 16 * 256));
          st16(dst + (ks * nbt + nb) * 1024 + lane * 16, __builtin_bit_cast(u32x4, f));
          c = mfma(ones, f, c);
        }
        if (cs && g == 0) cs[nb * 16 + lane] = c[0];
      }
    }
    STAMPEND(0);
    return;
  }
  if (wid >= 4) {
    const int w = wid - 4;
    unsigned tv[C::PT], wv[C::PW];
    const char* wb = ct < 10 ? wqkv + (640 + ct * 128) * 2 : w1 + (ct - 10) * 256;
    const int pitch = ct < 10 ? 3840 : 1280;
    make_tokv<C>(tv, w, lane, 0); make_wv<C>(wv, w, lane, pitch);
    float s[4] = {0.f, 0.f, 0.f, 0.f}, q[4] = {0.f, 0.f, 0.f, 0.f};
    LAS const char* my = lds + w * 1024 + lane * 16;
    auto post = [&](int j) {
      LAS const char* p = my + (j & 3) * C::STAGE;
#pragma unroll
      for (int i = 0; i < 4; i++) {
        u32x4 v = *(LAS const u32x4*)(p + i * 4096);
#pragma unroll
        for (int e = 0; e < 4; e++) { s[i] = dot2(v[e], 0x3f803f80u, s[i]); q[i] = dot2(v[e], v[e], q[i]); }
      }
    };
    auto fin = [&]() {
#pragma unroll
      for (int i = 0; i < 4; i++) {
#pragma unroll
        for (int m = 1; m < 8; m <<= 1) { s[i] += shx(s[i], m); q[i] += shx(q[i], m); }
      }
      const int i = lane & 3;
      const float sv = sel4(f32x4{s[0], s[1], s[2], s[3]}, i), qv = sel4(f32x4{q[0], q[1], q[2], q[3]}, i);
      const float m = sv * (1.0f / D_), var = fmaxf(qv * (1.0f / D_) - m * m, 0.f);
      const float rs = __builtin_amdgcn_rsqf(var + EPS);
      float r = rs;
      if (ct >= 10) r = rs * __builtin_amdgcn_rsqf(var * rs * rs + EPS);
      const int row = (i * 4 + w) * 8 + (lane >> 3);
      if ((lane & 7) < 4) {
        AUXF[row] = r; AUXF[128 + row] = r * m;
        if (ct == 0) ((f32x2*)(ws + OFF_ST))[rt * 128 + row] = f32x2{m, rs};
      }
      BAR();
    };
    loader<C, 0>(w, (unsigned)(size_t)lds, x + (size_t)rt * 128 * 1280, 128, tv, wb, 64 * pitch, wv, 10, post, Nop(), fin);
    waitvm<0>();
    STAMP(0, 1);
    return;
  }
  const int wm = wid >> 1, wn = wid & 1, g = lane >> 4, tk = lane & 15;
  CAddr<C> a; make_caddr<C>(a, wm, wn, lane, 0, 2 * wm);
  f32x4 acc[4][4]; zero(acc);
  Extra<2, 0, 0> ex; zero(ex);
  const bf16x8 ones = ones_frag();
  Frags<C, 4> fa;
  CBAR(); STAMP(0, 1); CSTAMP(0, 6); load_all<C, 4>(fa, lds, a, 0);
  if (ct < 10) consume<C, 4, false, true>(acc, ex, fa, lds, a, 0, 10, ones);
  else consume<C, 4, true, true>(acc, ex, fa, lds, a, 0, 10, ones);
  STAMP(0, 2); CSTAMP(0, 7);
  if (g == 0) {
#pragma unroll
    for (int i = 0; i < 2; i++) AUXF[256 + wn * 64 + (2 * wm + i) * 16 + tk] = ex.cs[i][0];
  }
  CBAR();
  STAMP(0, 3);
  if (ct < 10) {
    char* dst = ws + (ct < 5 ? OFF_PK : OFF_V) + lane * 16;
    const int cb0 = (ct < 5 ? ct : ct - 5) * 8 + wn * 4;
    float cs[4]; f32x2 zs[4];
#pragma unroll
    for (int j = 0; j < 4; j++) { cs[j] = AUXF[256 + wn * 64 + ((j + 2 * wm) & 3) * 16 + tk]; zs[j] = f32x2{0.f, 0.f}; }
#pragma unroll
    for (int ip = 0; ip < 2; ip++) {
      const int tb = wm * 64 + ip * 32 + 4 * g;
      f32x4 r0 = ldsf4(AUXF + tb), r1 = ldsf4(AUXF + tb + 16), m0 = ldsf4(AUXF + 128 + tb), m1 = ldsf4(AUXF + 128 + tb + 16);
      char* d2 = dst + (size_t)((rt * 4 + wm * 2 + ip) * 40 + cb0) * 1024;
#pragma unroll
      for (int j = 0; j < 4; j++) {
        f32x4 t0 = r0 * acc[2 * ip][j] - m0 * cs[j], t1 = r1 * acc[2 * ip + 1][j] - m1 * cs[j];
        u32x4 o;
        if (ct < 5) {
          o = u32x4{pk2v(phi2(lo2(t0))), pk2v(phi2(hi2(t0))), pk2v(phi2(lo2(t1))), pk2v(phi2(hi2(t1)))};
          zs[j] += (unpk(o[0]) + unpk(o[1])) + (unpk(o[2]) + unpk(o[3]));
        } else {
          o = u32x4{pk2v(lo2(t0)), pk2v(hi2(t0)), pk2v(lo2(t1)), pk2v(hi2(t1))};
        }
        st16(d2 + ((j + 2 * wm) & 3) * 1024, o);
      }
    }
    if (ct < 5) {
      float z[4];
#pragma unroll
      for (int j = 0; j < 4; j++) { z[j] = zs[j][0] + zs[j][1]; z[j] += shx(z[j], 16); z[j] += shx(z[j], 32); }
      if (wm == 1 && lane < 16) {
#pragma unroll
        for (int j = 0; j < 4; j++) AUXF[704 + wn * 64 + ((j + 2) & 3) * 16 + lane] = z[j];
      }
      CBAR();
      if (wm == 0 && lane < 16) {
        float* zp = (float*)(ws + OFF_ZP) + rt * 640 + ct * 128 + wn * 64;
#pragma unroll
        for (int j = 0; j < 4; j++) zp[j * 16 + lane] = z[j] + AUXF[704 + wn * 64 + j * 16 + lane];
      }
    }
  } else {
    const int c0 = (ct - 10) * 128 + wn * 64;
#pragma unroll
    for (int i = 0; i < 4; i++) {
      const int row = wm * 64 + i * 16 + tk;
      const float rr = AUXF[row], rm = AUXF[128 + row];
#pragma unroll
      for (int jp = 0; jp < 2; jp++) {
        const int jt = jp ^ wm;
        u32x4 o;
#pragma unroll
        for (int h = 0; h < 2; h++) {
          f32x4 cs = ldsf4(AUXF + 256 + wn * 64 + (2 * jt + h) * 16 + 4 * g);
          f32x4 t = rr * acc[i][2 * jp + h] - rm * cs;
          o[2 * h] = pk2v(gelu2(lo2(t))); o[2 * h + 1] = pk2v(gelu2(hi2(t)));
        }
        const int col = c0 + jt * 32, grp = col / 160;
        const int dl = grp == 0 ? 1 : (grp == 1 ? -1 : (grp == 2 ? 2 : -2));
        int td = rt * 128 + row + dl;
        const bool ok = td >= 0 && td < S_;
        td &= S_ - 1;
        if (!ok) o = u32x4{0, 0, 0, 0};
        st16(ws + OFF_H + ((size_t)((td >> 4) * 20 + (col >> 5)) * 64 + g * 16 + (td & 15)) * 16, o);
      }
    }
  }
  waitvm<0>();
  STAMPEND(0);
}

__global__ __launch_bounds__(512) void phase2(const char* __restrict__ x, const char* __restrict__ wo, char* __restrict__ ws) {
  const int b = blockIdx.x;
  const int wid = __builtin_amdgcn_readfirstlane(threadIdx.x >> 6), lane = threadIdx.x & 63;
  LAS char* lds = (LAS char*)smem;
  const int g = lane >> 4, tk = lane & 15;
  STAMP(1, 0);
  if (b < 128) {
    typedef CfgQ C;
    const int rt = b >> 2, ct = b & 3;
    if (wid >= 4) {
      const int w = wid - 4;
      unsigned tv[C::PT], wv[C::PW];
      make_tokv<C>(tv, w, lane, 0); make_wv<C>(wv, w, lane, 40);
      loader<C, 0>(w, (unsigned)(size_t)lds, x + (size_t)rt * 64 * 1280, 128, tv, ws + OFF_WQ + ct * 10 * 1024, 80 * 1024, wv, 10, Nop(), Nop(), Nop());
      STAMP(1, 1);
      return;
    }
    const int wm = wid >> 1, wn = wid & 1;
    float zp[16], cp[10];
    const int t = threadIdx.x;
    if (t < 160) {
      const float* p = (const float*)(ws + OFF_ZP) + ct * 160 + t;
#pragma unroll
      for (int r = 0; r < 16; r++) zp[r] = p[r * 640];
      const float* pc = (const float*)(ws + OFF_CSQ) + ct * 160 + t;
#pragma unroll
      for (int r = 0; r < 10; r++) cp[r] = pc[r * 640];
    }
    f32x2 st[2];
#pragma unroll
    for (int i = 0; i < 2; i++) st[i] = ((const f32x2*)(ws + OFF_ST))[rt * 64 + wm * 32 + i * 16 + tk];
    CAddr<C> a; make_caddr<C>(a, wm, wn, lane);
    f32x4 acc[2][5]; zero(acc);
    Extra<0, 0, 0> ex;
    const bf16x8 ones = ones_frag();
    Frags<C, 5> fa;
    CBAR(); STAMP(1, 1); load_all<C, 5>(fa, lds, a, 0);
    if (t < 160) {
      float z = 0.f, c = 0.f;
#pragma unroll
      for (int r = 0; r < 16; r += 4) z += (zp[r] + zp[r + 1]) + (zp[r + 2] + zp[r + 3]);
#pragma unroll
      for (int r = 0; r < 10; r += 2) c += cp[r] + cp[r + 1];
      AUXF[512 + t] = z; AUXF[256 + t] = c;
    }
    consume<C, 5, true, true>(acc, ex, fa, lds, a, 0, 10, ones);
    STAMP(1, 2);
#pragma unroll
    for (int i = 0; i < 2; i++) {
      const float rs = st[i][1], rm = rs * st[i][0];
      f32x4 pq[5]; float part[5];
#pragma unroll
      for (int j = 0; j < 5; j++) {
        f32x4 z = ldsf4(AUXF + 512 + wn * 80 + j * 16 + 4 * g), cs = ldsf4(AUXF + 256 + wn * 80 + j * 16 + 4 * g);
        f32x4 tt = rs * acc[i][j] - rm * cs;
        f32x2 p0 = phi2(lo2(tt)), p1 = phi2(hi2(tt));
        pq[j] = f32x4{p0[0], p0[1], p1[0], p1[1]};
        f32x2 s = p0 * lo2(z) + p1 * hi2(z);
        part[j] = s[0] + s[1];
      }
      float dh[4];
      dh[0] = part[0] + (g == 0 ? part[1] : 0.f);
      dh[1] = (g >= 1 ? part[1] : 0.f) + (g <= 1 ? part[2] : 0.f);
      dh[2] = (g >= 2 ? part[2] : 0.f) + (g <= 2 ? part[3] : 0.f);
      dh[3] = (g == 3 ? part[3] : 0.f) + part[4];
#pragma unroll
      for (int h = 0; h < 4; h++) { dh[h] += shx(dh[h], 16); dh[h] += shx(dh[h], 32); dh[h] = rcp(dh[h] + 1e-6f); }
      const float sc[5] = {dh[0], g == 0 ? dh[0] : dh[1], g <= 1 ? dh[1] : dh[2], g <= 2 ? dh[2] : dh[3], dh[3]};
#pragma unroll
      for (int j = 0; j < 5; j++) pq[j] *= sc[j];
      char* dst = ws + OFF_PQ + ((size_t)(rt * 4 + wm * 2 + i) * 20 + ct * 5) * 1024 + lane * 16;
      st16(dst + (3 * wn) * 1024, u32x4{pk2v(lo2(pq[0])), pk2v(hi2(pq[0])), pk2v(lo2(pq[1])), pk2v(hi2(pq[1]))});
      st16(dst + (3 * wn + 1) * 1024, u32x4{pk2v(lo2(pq[2])), pk2v(hi2(pq[2])), pk2v(lo2(pq[3])), pk2v(hi2(pq[3]))});
      *(u32x2*)(dst + 2 * 1024 + wn * 8) = u32x2{pk2v(lo2(pq[4])), pk2v(hi2(pq[4]))};
    }
    STAMPEND(1);
  } else if (b < 208) {
    typedef CfgA C;
    const int bb = b - 128, rt = (bb & 7) * 2 + (bb >> 3) / 5, ct = (bb >> 3) % 5;
    if (wid >= 4) {
      const int w = wid - 4;
      unsigned tv[C::PT], wv[C::PW];
      make_tokv<C>(tv, w, lane, 20); make_wv<C>(wv, w, lane, 40);
      const char* xb = x + (size_t)rt * 128 * 1280 + ct * 256;
      const unsigned xr = w * 4 + g, xo = xr * 1280 + ((tk ^ xr) << 4);
      auto ext = [&](unsigned dst) {
#pragma unroll
        for (int i = 0; i < 8; i++) dma16(xb, xo + i * 16 * 1280, dst + (i * 4 + w) * 1024);
      };
      auto fin = [&]() { waitvm<0>(); BAR(); };
      loader<C, 8>(w, (unsigned)(size_t)lds, ws + OFF_H + (size_t)rt * 8 * 20 * 1024, 2048, tv, ws + OFF_W2 + ct * 8 * 1024, 80 * 1024, wv, 10, Nop(), ext, fin);
      STAMP(1, 1);
      return;
    }
    const int wm = wid >> 1, wn = wid & 1;
    f32x2 st[4];
#pragma unroll
    for (int i = 0; i < 4; i++) st[i] = ((const f32x2*)(ws + OFF_ST))[rt * 128 + wm * 64 + i * 16 + tk];
    CAddr<C> a; make_caddr<C>(a, wm, wn, lane);
    f32x4 acc[4][4]; zero(acc);
    Extra<0, 0, 0> ex;
    const bf16x8 ones = ones_frag();
    Frags<C, 4> fa;
    CBAR(); STAMP(1, 1); load_all<C, 4>(fa, lds, a, 0);
    consume<C, 4, true, true>(acc, ex, fa, lds, a, 0, 10, ones);
    STAMP(1, 2);
    CBAR();
    LAS const char* xs = lds + 2 * C::STAGE + (wm * 64 + tk) * 256 + (g & 1) * 8;
#pragma unroll
    for (int i = 0; i < 4; i++) {
      const float rs = st[i][1], rm = st[i][0] * rs;
      f32x2 s1 = {0.f, 0.f}, s2 = {0.f, 0.f};
      char* dst = ws + OFF_X1 + ((size_t)(rt * 8 + wm * 4 + i) * 20 + ct * 4 + wn * 2) * 1024 + lane * 16;
#pragma unroll
      for (int jp = 0; jp < 2; jp++) {
        u32x4 o;
#pragma unroll
        for (int h = 0; h < 2; h++) {
          u32x2 xx = *(LAS const u32x2*)(xs + i * 16 * 256 + (((wn * 8 + (2 * jp + h) * 2 + (g >> 1)) ^ tk) << 4));
          f32x4 c = acc[i][2 * jp + h] - rm;
          o[2 * h] = pk2v(unpk(xx[0]) * rs + lo2(c));
          o[2 * h + 1] = pk2v(unpk(xx[1]) * rs + hi2(c));
        }
#pragma unroll
        for (int e = 0; e < 4; e++) { f32x2 v = unpk(o[e]); s1 += v; s2 += v * v; }
        st16(dst + jp * 1024, o);
      }
      float a1 = s1[0] + s1[1], a2 = s2[0] + s2[1];
      a1 += shx(a1, 16); a2 += shx(a2, 16); a1 += shx(a1, 32); a2 += shx(a2, 32);
      if (g == 0) ((f32x2*)(ws + OFF_XS))[(ct * 2 + wn) * 2048 + rt * 128 + wm * 64 + i * 16 + tk] = f32x2{a1, a2};
    }
    STAMPEND(1);
  } else {
    const int bb = b - 208, G = bb & 7, R = bb >> 3, t = threadIdx.x;
    LAS char* wt = lds + 106496;
    {
      const char* wb = wo + (size_t)G * 80 * 1280 + R * 320;
#pragma unroll
      for (int i = 0; i < 4; i++) {
        const unsigned pc = min(i * 8 + wid, 29), s = pc * 64 + lane, row = min(s / 20, 79u);
        dma16(wb, row * 1280 + (s % 20) * 16, (unsigned)(size_t)wt + pc * 1024);
      }
    }
    f32x4 acc[13];
#pragma unroll
    for (int i = 0; i < 13; i++) acc[i] = f32x4{0.f, 0.f, 0.f, 0.f};
    bf16x8 pk[2][5], vv[2][5];
#pragma unroll
    for (int u = 0; u < 2; u++)
#pragma unroll
      for (int j = 0; j < 5; j++) {
        size_t o = ((size_t)(R * 16 + wid * 2 + u) * 40 + G * 5 + j) * 1024 + lane * 16;
        pk[u][j] = *(const bf16x8*)(ws + OFF_PK + o); vv[u][j] = *(const bf16x8*)(ws + OFF_V + o);
      }
#pragma unroll
    for (int u = 0; u < 2; u++)
#pragma unroll
      for (int db = 0; db < 5; db++)
#pragma unroll
        for (int eb = db - 1; eb <= db + 1; eb++)
          if (eb >= 0 && eb < 5) acc[2 * db + eb] = mfma(vv[u][eb], pk[u][db], acc[2 * db + eb]);
    STAMP(1, 2);
#pragma unroll
    for (int i = 0; i < 13; i++) *(LAS f32x4*)(lds + (wid * 13 + i) * 1024 + lane * 16) = acc[i];
    __syncthreads();
    for (int o = t; o < 832; o += 512) {
      f32x4 s = *(LAS const f32x4*)(lds + o * 16);
#pragma unroll
      for (int w = 1; w < 8; w++) s += *(LAS const f32x4*)(lds + w * 13 * 1024 + o * 16);
      st16wt(ws + OFF_KVP + (size_t)(R * 8 + G) * 13 * 1024 + o * 16, __builtin_bit_cast(u32x4, s));
    }
    waitvm<0>();
    __syncthreads();
    unsigned* cnt = (unsigned*)(ws + OFF_FLAG) + G * 64;
    if (t == 0) {
      __hip_atomic_fetch_add(cnt, 1u, RLX);
      for (int it = 0; it < (1 << 20) && __hip_atomic_load(cnt, RLX) < 4u; it++) __builtin_amdgcn_s_sleep(1);
    }
    __syncthreads();
    __builtin_amdgcn_fence(__ATOMIC_ACQUIRE, "agent");
    STAMP(1, 3);
    for (int o = t; o < 832; o += 512) {
      const f32x4* p = (const f32x4*)(ws + OFF_KVP + (size_t)G * 13 * 1024) + o;
      f32x4 s = (p[0] + p[8 * 13 * 64]) + (p[2 * 8 * 13 * 64] + p[3 * 8 * 13 * 64]);
      const int i = o >> 6, l = o & 63, db = (i + 1) / 3, eb = i - 2 * db;
      const int hd = (db * 16 + (l & 15)) / 20, e0 = eb * 16 + (l >> 4) * 4;
#pragma unroll
      for (int r = 0; r < 4; r++) if ((e0 + r) / 20 != hd) s[r] = 0.f;
      *(LAS u32x2*)(lds + o * 8) = u32x2{pk2v(lo2(s)), pk2v(hi2(s))};
    }
    __syncthreads();
    {
      const int q = (lane >> 2) & 3, p = lane & 3;
      for (int f = wid; f < 50; f += 8) {
        const int db = f / 10, nb = f % 10;
        f32x4 c = {0.f, 0.f, 0.f, 0.f};
#pragma unroll
        for (int kk = 0; kk < 2; kk++) {
          const int ks = kk + (db >= 3 ? 1 : 0);
          u32x2 h[2];
#pragma unroll
          for (int e = 0; e < 2; e++) {
            const int eb = 2 * ks + e;
            const bool in = eb >= db - 1 && eb <= db + 1 && eb < 5;
            h[e] = *(LAS const u32x2*)(lds + ((in ? 2 * db + eb : 0) * 64 + lane) * 8);
            if (!in) h[e] = u32x2{0u, 0u};
          }
          LAS const char* src = wt + (ks * 32 + 4 * g + q) * 320 + nb * 32 + p * 8;
          c = mfma(__builtin_bit_cast(bf16x8, u32x4{h[0][0], h[0][1], h[1][0], h[1][1]}), cat(ldstr(src), ldstr(src + 16 * 320)), c);
        }
        const int beta = 5 * G + db, GG = beta / 10, j = beta % 10, wn = j / 5, jj = j % 5;
        const int kap = jj == 4 ? 5 * GG + 2 : 5 * GG + 3 * wn + (jj >> 1), tt = jj == 4 ? wn : (jj & 1);
        *(u32x2*)(ws + OFF_ME + ((size_t)kap * 40 + R * 10 + nb) * 1024 + lane * 16 + tt * 8) = u32x2{pk2v(lo2(c)), pk2v(hi2(c))};
      }
    }
    STAMPEND(1);
  }
}

__global__ __launch_bounds__(512) void phase3(char* __restrict__ ws) {
  const int b = blockIdx.x, xcd = b & 7;
  const int wid = __builtin_amdgcn_readfirstlane(threadIdx.x >> 6), lane = threadIdx.x & 63;
  LAS char* lds = (LAS char*)smem;
  const int wm = (wid >> 1) & 1, wn = wid & 1, g = lane >> 4, tk = lane & 15;
  STAMP(2, 0);
  if (b < 192) {
    typedef CfgA C;
    const int idx = b >> 3;
    const int rt = (xcd >> 1) * 4 + (idx & 3), ct = (xcd & 1) * 6 + (idx >> 2);
    if (wid >= 4) {
      const int w = wid - 4;
      unsigned tv[C::PT], wv[C::PW];
      make_tokv<C>(tv, w, lane, 20); make_wv<C>(wv, w, lane, 96);
      loader<C, 0>(w, (unsigned)(size_t)lds, ws + OFF_X1 + (size_t)rt * 8 * 20 * 1024, 2048, tv, ws + OFF_W3 + ct * 8 * 1024, 192 * 1024, wv, 10, Nop(), Nop(), Nop());
      STAMP(2, 1);
      return;
    }
    f32x2 xs[10];
    if (wid < 2) {
      const f32x2* p = (const f32x2*)(ws + OFF_XS) + rt * 128 + threadIdx.x;
#pragma unroll
      for (int i = 0; i < 10; i++) xs[i] = p[i * 2048];
    } else {
      const float* p = (const float*)(ws + OFF_CS3) + ct * 128 + (threadIdx.x - 128);
#pragma unroll
      for (int i = 0; i < 10; i++) xs[i][0] = p[i * 1536];
    }
    CAddr<C> a; make_caddr<C>(a, wm, wn, lane);
    f32x4 acc[4][4]; zero(acc);
    Extra<0, 0, 0> ex;
    const bf16x8 ones = ones_frag();
    Frags<C, 4> fa;
    CBAR(); STAMP(2, 1); load_all<C, 4>(fa, lds, a, 0);
    if (wid < 2) {
      f32x2 s = ((xs[0] + xs[1]) + (xs[2] + xs[3])) + ((xs[4] + xs[5]) + (xs[6] + xs[7])) + (xs[8] + xs[9]);
      float m = s[0] * (1.0f / D_), var = fmaxf(s[1] * (1.0f / D_) - m * m, 0.f);
      float rs = __builtin_amdgcn_rsqf(var + EPS);
      AUXF[threadIdx.x] = rs; AUXF[128 + threadIdx.x] = rs * m;
    } else {
      AUXF[128 + threadIdx.x] = ((xs[0][0] + xs[1][0]) + (xs[2][0] + xs[3][0])) + ((xs[4][0] + xs[5][0]) + (xs[6][0] + xs[7][0])) + (xs[8][0] + xs[9][0]);
    }
    consume<C, 4, true, true>(acc, ex, fa, lds, a, 0, 10, ones);
    STAMP(2, 2);
#pragma unroll
    for (int i = 0; i < 4; i++) {
      const int row = wm * 64 + i * 16 + tk;
      const float rs = AUXF[row], rm = AUXF[128 + row];
      char* dst = ws + OFF_H3 + ((size_t)(rt * 8 + wm * 4 + i) * 48 + ct * 4 + wn * 2) * 1024 + lane * 16;
#pragma unroll
      for (int jp = 0; jp < 2; jp++) {
        u32x4 o;
#pragma unroll
        for (int h = 0; h < 2; h++) {
          f32x4 cs = ldsf4(AUXF + 256 + wn * 64 + (2 * jp + h) * 16 + 4 * g);
          f32x4 t = rs * acc[i][2 * jp + h] - rm * cs;
          o[2 * h] = pk2v(gelu2(lo2(t))); o[2 * h + 1] = pk2v(gelu2(hi2(t)));
        }
        st16(dst + jp * 1024, o);
      }
    }
    STAMPEND(2);
  } else {
    typedef CfgS C;
    const int idx = (b - 192) >> 3;
    const int rt = (xcd >> 1) * 4 + (idx & 3), ct = (xcd & 1) * 2 + (idx >> 2);
    if (wid >= 4) {
      const int w = wid - 4;
      unsigned tv[C::PT], wv[C::PW];
      make_tokv<C>(tv, w, lane, 20); make_wv<C>(wv, w, lane, 40);
      loader<C, 0>(w, (unsigned)(size_t)lds, ws + OFF_PQ + (size_t)rt * 8 * 20 * 1024, 2048, tv, ws + OFF_ME + ct * 10 * 1024, 80 * 1024, wv, 10, Nop(), Nop(), Nop());
      STAMP(2, 1);
      return;
    }
    CAddr<C> a; make_caddr<C>(a, wm, wn, lane);
    f32x4 acc[4][5]; zero(acc);
    Extra<0, 0, 0> ex;
    const bf16x8 ones = ones_frag();
    Frags<C, 5> fa;
    CBAR(); STAMP(2, 1); load_all<C, 5>(fa, lds, a, 0);
    consume<C, 5, true, true>(acc, ex, fa, lds, a, 0, 10, ones);
    STAMP(2, 2);
#pragma unroll
    for (int i = 0; i < 4; i++) {
      char* dst = ws + OFF_SA + ((size_t)(rt * 8 + wm * 4 + i) * 20 + ct * 5) * 1024 + lane * 16;
      u32x2 o[5];
#pragma unroll
      for (int j = 0; j < 5; j++) o[j] = u32x2{pk2v(lo2(acc[i][j])), pk2v(hi2(acc[i][j]))};
      if (wn == 0) {
        st16(dst, u32x4{o[0][0], o[0][1], o[1][0], o[1][1]});
        st16(dst + 1024, u32x4{o[2][0], o[2][1], o[3][0], o[3][1]});
        *(u32x2*)(dst + 2048) = o[4];
      } else {
        *(u32x2*)(dst + 2048 + 8) = o[0];
        st16(dst + 3072, u32x4{o[1][0], o[1][1], o[2][0], o[2][1]});
        st16(dst + 4096, u32x4{o[3][0], o[3][1], o[4][0], o[4][1]});
      }
    }
    STAMPEND(2);
  }
}

template <int NBW>
DI void p4_consumer(char* __restrict__ out, char* __restrict__ ws, LAS char* lds, int rt, int ct, int wm, int wn, int lane) {
  typedef CfgC C;
  const int g = lane >> 4, tk = lane & 15;
  u32x2 sav[2][NBW], x1v[2][NBW];
  CAddr<C> a; make_caddr<C>(a, wm, wn, lane);
  f32x4 h2[2][NBW]; zero(h2);
  Extra<0, 0, 0> ex;
  const bf16x8 ones = ones_frag();
  Frags<C, NBW> fa;
  CBAR(); STAMP(3, 1); load_all<C, NBW>(fa, lds, a, 0);
  consume<C, NBW, true, false>(h2, ex, fa, lds, a, 0, 9, ones);
#pragma unroll
  for (int i = 0; i < 2; i++)
#pragma unroll
    for (int j = 0; j < NBW; j++) {
      const int blk = ct * 5 + wn * 3 + j;
      const size_t o = ((size_t)(rt * 4 + wm * 2 + i) * 20 + (blk >> 1)) * 1024 + lane * 16 + (blk & 1) * 8;
      x1v[i][j] = *(const u32x2*)(ws + OFF_X1 + o);
      sav[i][j] = *(const u32x2*)(ws + OFF_SA + o);
    }
  consume<C, NBW, true, true>(h2, ex, fa, lds, a, 9, 12, ones);
  STAMP(3, 2);
  CBAR();
  LAS char* xs = lds + (wm * 32 + tk) * 160 + wn * 96 + g * 8;
#pragma unroll
  for (int i = 0; i < 2; i++)
#pragma unroll
    for (int j = 0; j < NBW; j++) {
      LAS u32x2* px = (LAS u32x2*)(xs + i * 16 * 160 + j * 32);
      const u32x2 xv = *px;
      u32x2 o;
#pragma unroll
      for (int e = 0; e < 2; e++) {
        f32x2 bb = unpk(x1v[i][j][e]) + (e ? hi2(h2[i][j]) : lo2(h2[i][j]));
        f32x2 z = bb * -1.4426950408889634f;
        f32x2 d = f32x2{ex2(z[0]), ex2(z[1])} + 1.0f;
        f32x2 sl = bb * f32x2{rcp(d[0]), rcp(d[1])};
        o[e] = pk2v(unpk(xv[e]) + unpk(sav[i][j][e]) * sl);
      }
      *px = o;
    }
  CBAR();
  const int t = (wm * 2 + wn) * 64 + lane;
#pragma unroll
  for (int i = 0; i < 3; i++) {
    const int ch = t + 256 * i;
    if (ch < 640) {
      const int row = ch / 10, c = ch % 10;
      *(u32x4*)(out + (size_t)(rt * 64 + row) * 1280 + ct * 160 + c * 16) = *(LAS const u32x4*)(lds + ch * 16);
    }
  }
  STAMPEND(3);
}
__global__ __launch_bounds__(512) void phase4(const char* __restrict__ x, char* __restrict__ ws, char* __restrict__ out) {
  typedef CfgC C;
  const int b = blockIdx.x, xcd = b & 7, idx = b >> 3;
  const int rt = (xcd >> 1) * 8 + (idx & 7), ct = (xcd & 1) * 4 + (idx >> 3);
  const int wid = __builtin_amdgcn_readfirstlane(threadIdx.x >> 6), lane = threadIdx.x & 63;
  LAS char* lds = (LAS char*)smem;
  STAMP(3, 0);
  if (wid >= 4) {
    const int w = wid - 4;
    unsigned tv[C::PT], wv[C::PW];
    make_tokv<C>(tv, w, lane, 48); make_wv<C>(wv, w, lane, 40);
    const char* xb = x + (size_t)rt * 64 * 1280 + ct * 160;
    auto ext = [&](unsigned dst) {
#pragma unroll
      for (int i = 0; i < 3; i++) {
        const unsigned ch = min((i * 4 + w) * 64 + lane, 639);
        dma16(xb, (ch / 10) * 1280 + (ch % 10) * 16, dst + (i * 4 + w) * 1024);
      }
    };
    auto fin = [&]() { waitvm<0>(); BAR(); };
    loader<C, 3>(w, (unsigned)(size_t)lds, ws + OFF_H3 + (size_t)rt * 4 * 48 * 1024, 4096, tv, ws + OFF_W4 + ct * 5 * 1024, 4 * 40 * 1024, wv, 12, Nop(), ext, fin);
    STAMP(3, 1);
    return;
  }
  const int wm = wid >> 1, wn = wid & 1;
  if (wn == 0) p4_consumer<3>(out, ws, lds, rt, ct, wm, wn, lane);
  else p4_consumer<2>(out, ws, lds, rt, ct, wm, wn, lane);
}

extern "C" void kernel_launch(void* const* d_in, const int* in_sizes, int n_in, void* d_out, int out_size, void* d_ws, size_t ws_size, hipStream_t stream) {
  const char* x = (const char*)d_in[0]; const char* wqkv = (const char*)d_in[1]; const char* wo = (const char*)d_in[2];
  const char* w1 = (const char*)d_in[4]; const char* w2 = (const char*)d_in[5]; const char* w3 = (const char*)d_in[6]; const char* w4 = (const char*)d_in[7];
  char* ws = (char*)d_ws;
  (void)hipFuncSetAttribute((const void*)phase1, hipFuncAttributeMaxDynamicSharedMemorySize, LDS_TOTAL);
  (void)hipFuncSetAttribute((const void*)phase2, hipFuncAttributeMaxDynamicSharedMemorySize, LDS_TOTAL);
  (void)hipFuncSetAttribute((const void*)phase3, hipFuncAttributeMaxDynamicSharedMemorySize, LDS_TOTAL);
  (void)hipFuncSetAttribute((const void*)phase4, hipFuncAttributeMaxDynamicSharedMemorySize, LDS_TOTAL);
  phase1<<<256, 512, LDS_TOTAL, stream>>>(x, wqkv, w1, ws, wo, w2, w3, w4);
  phase2<<<240, 512, LDS_TOTAL, stream>>>(x, wo, ws);
  phase3<<<256, 512, LDS_TOTAL, stream>>>(ws);
  phase4<<<256, 512, LDS_TOTAL, stream>>>(x, ws, (char*)d_out);
}
```

```cpp
#include <hip/hip_runtime.h>
#include <stdint.h>

typedef __attribute__((ext_vector_type(8))) __bf16 bf16x8;
typedef __attribute__((ext_vector_type(2))) __bf16 bf16x2;
typedef __attribute__((ext_vector_type(4))) float f32x4;
typedef __attribute__((ext_vector_type(2))) float f32x2;
typedef __attribute__((ext_vector_type(4))) short v4i16;
typedef __attribute__((ext_vector_type(8))) short v8i16;
typedef __attribute__((ext_vector_type(4))) unsigned u32x4;
typedef __attribute__((ext_vector_type(2))) unsigned u32x2;
#define LAS __attribute__((address_space(3)))
#define DI __device__ __forceinline__
#define SB() __builtin_amdgcn_sched_barrier(0)

constexpr int S_ = 2048, D_ = 640, F_ = 1536;
constexpr size_t KB = 1024;
constexpr size_t OFF_PK = 0;
constexpr size_t OFF_V = OFF_PK + 64 * 40 * KB;
constexpr size_t OFF_H = OFF_V + 64 * 40 * KB;
constexpr size_t OFF_X1 = OFF_H + 128 * 20 * KB;
constexpr size_t OFF_H3 = OFF_X1 + 128 * 20 * KB;
constexpr size_t OFF_PQ = OFF_H3 + 128 * 48 * KB;
constexpr size_t OFF_SA = OFF_PQ + 128 * 20 * KB;
constexpr size_t OFF_W4 = OFF_SA + 128 * 20 * KB;
constexpr size_t OFF_ME = OFF_W4 + 48 * 40 * KB;
constexpr size_t OFF_ZP = OFF_ME + 20 * 40 * KB;
constexpr size_t OFF_ST = OFF_ZP + 16 * 640 * 4;
constexpr size_t OFF_XS = OFF_ST + 2048 * 8;
constexpr float EPS = 1e-5f;
#define STAMP(k, i)
#define STAMPEND(k)
#define CSTAMP(k, i)

DI void dma16(const void* sbase, unsigned voff, unsigned ldsdst) {
  unsigned keep;
  asm volatile("s_mov_b32 %0, m0\n\ts_mov_b32 m0, %3\n\ts_nop 0\n\tglobal_load_lds_dwordx4 %1, %2\n\ts_mov_b32 m0, %0"
               : "=&s"(keep) : "v"(voff), "s"(sbase), "s"(ldsdst) : "memory");
}
DI void dma4(const void* sbase, unsigned voff, unsigned ldsdst) {
  unsigned keep;
  asm volatile("s_mov_b32 %0, m0\n\ts_mov_b32 m0, %3\n\ts_nop 0\n\tglobal_load_lds_dword %1, %2\n\ts_mov_b32 m0, %0"
               : "=&s"(keep) : "v"(voff), "s"(sbase), "s"(ldsdst) : "memory");
}
#define CODE_ADDR(sym, out) asm volatile("s_getpc_b64 s[40:41]\n\ts_add_u32 s40, s40, " sym "@rel32@lo+4\n\ts_addc_u32 s41, s41, " sym "@rel32@hi+12\n\ts_mov_b64 %0, s[40:41]" \
                                         : "=s"(out) :: "s40", "s41", "scc")
constexpr unsigned P4LEN = 9088;
constexpr int XC = 1;
DI void touch_code(unsigned gid, unsigned lane, unsigned ldsdst) {
  constexpr int dummy = 0; (void)dummy;
  unsigned long long a, b;
  CODE_ADDR("k_phase1", a); CODE_ADDR("k_phase4", b);
  const unsigned len = min((unsigned)(b - a) + P4LEN, 131072u), o = (gid * 64 + lane) * 128;
  dma4((const char*)a, min(o, len - 128), ldsdst);
}
DI void st16(void* p, u32x4 v) { *(u32x4*)p = v; }
DI void st16wt(void* p, u32x4 v) { asm volatile("global_store_dwordx4 %0, %1, off sc1\n\ts_nop 1" ::"v"(p), "v"(v) : "memory"); }
DI float dot2(unsigned a, unsigned b, float c) { return __builtin_amdgcn_fdot2_f32_bf16(__builtin_bit_cast(bf16x2, a), __builtin_bit_cast(bf16x2, b), c, false); }
#define BAR() asm volatile("s_waitcnt lgkmcnt(0)\n\ts_barrier" ::: "memory")
template <int N> DI void waitvm() { asm volatile("s_waitcnt vmcnt(%0)" ::"n"(N) : "memory"); }
DI unsigned pk2(float lo, float hi) { f32x2 v = {lo, hi}; bf16x2 b = __builtin_convertvector(v, bf16x2); return __builtin_bit_cast(unsigned, b); }
DI unsigned pk2v(f32x2 v) { bf16x2 b = __builtin_convertvector(v, bf16x2); return __builtin_bit_cast(unsigned, b); }
DI float bflo(unsigned u) { return __uint_as_float(u << 16); }
DI float bfhi(unsigned u) { return __uint_as_float(u & 0xffff0000u); }
DI f32x2 unpk(unsigned u) { return f32x2{bflo(u), bfhi(u)}; }
DI float ex2(float x) { return __builtin_amdgcn_exp2f(x); }
DI float rcp(float x) { return __builtin_amdgcn_rcpf(x); }
DI f32x2 lo2(f32x4 v) { return f32x2{v[0], v[1]}; }
DI f32x2 hi2(f32x4 v) { return f32x2{v[2], v[3]}; }
DI f32x2 gelu2(f32x2 t) {
  const float c1 = -1.5957691216057308f * 1.4426950408889634f, c2 = c1 * 0.044715f;
  f32x2 u = t * t;
  f32x2 z = t * (u * c2 + c1);
  f32x2 d = f32x2{ex2(z[0]), ex2(z[1])} + 1.0f;
  return t * f32x2{rcp(d[0]), rcp(d[1])};
}
DI f32x2 phi2(f32x2 t) {
  f32x2 a = t + (1.0f + 1e-6f), z = t * 1.4426950408889634f;
  f32x2 e = f32x2{ex2(z[0]), ex2(z[1])} + 1e-6f;
  return f32x2{t[0] > 0.f ? a[0] : e[0], t[1] > 0.f ? a[1] : e[1]};
}
DI float shx(float v, int m) { return __shfl_xor(v, m, 64); }
DI bf16x8 lds128(LAS const char* p) { return *(LAS const bf16x8*)p; }
DI v4i16 ldstr(LAS const char* p) { return __builtin_amdgcn_ds_read_tr16_b64_v4i16((LAS v4i16*)p); }
DI bf16x8 cat(v4i16 a, v4i16 b) { v8i16 r = __builtin_shufflevector(a, b, 0, 1, 2, 3, 4, 5, 6, 7); return __builtin_bit_cast(bf16x8, r); }
DI f32x4 mfma(bf16x8 a, bf16x8 b, f32x4 c) { return __builtin_amdgcn_mfma_f32_16x16x32_bf16(a, b, c, 0, 0, 0); }

template <int MB_, int NB_, int NB0_, int KS_, int D_R, bool XROW_, bool FRAGW_>
struct Cfg {
  static constexpr int MB = MB_, NB = NB_, NB0 = NB0_, KS = KS_, D = D_R;
  static constexpr bool XROW = XROW_, FRAGW = FRAGW_;
  static constexpr int MBW = MB / 2, RB = NB * 32, TOKB = MB * KS * 1024, WB = KS * 32 * RB, STAGE = TOKB + WB;
  static constexpr int PT = TOKB / 4096, PW = WB / 4096, P = PT + PW;
};
typedef Cfg<8, 8, 4, 2, 4, true, false> CfgA1;
typedef Cfg<8, 8, 4, 2, 4, false, false> CfgA;
typedef Cfg<4, 10, 5, 2, 4, true, false> CfgQ;
typedef Cfg<8, 10, 5, 2, 4, false, true> CfgS;
typedef Cfg<4, 5, 3, 4, 4, false, true> CfgC;
constexpr int LDS_AUX = 147456;
constexpr int LDS_TOTAL = LDS_AUX + 4096;
#define RLX __ATOMIC_RELAXED, __HIP_MEMORY_SCOPE_AGENT

template <int RB> DI unsigned wswz(unsigned rho) { return RB == 256 ? 2 * (rho & 7) : (RB == 320 ? 2 * ((rho >> 2) & 1) : 0); }

template <class C, int XT, int XE, class Pre, class Post, class Ext, class Fin>
DI void loader(int w, unsigned lds0, const char* tokbase, int tokstride, const unsigned (&tokv)[C::PT],
               const char* wb, int wstride, const unsigned (&wv)[C::PW], int NS, Pre pre, Post post, Ext ext, Fin fin) {
  static_assert(C::D == 4, "ring depth");
  auto issue = [&](int s) {
    const char* tb = tokbase + (size_t)s * tokstride;
    const char* wp = wb + (size_t)s * wstride;
    unsigned dst = lds0 + (s & 3) * C::STAGE + w * 1024;
#pragma unroll
    for (int i = 0; i < C::PT; i++) dma16(tb, tokv[i], dst + i * 4096);
#pragma unroll
    for (int i = 0; i < C::PW; i++) dma16(wp, wv[i], dst + C::TOKB + i * 4096);
  };
  issue(0); issue(1); pre();
  waitvm<C::P + XT>(); BAR();
  issue(2); issue(3); post(0);
  waitvm<2 * C::P + XT>(); BAR();
  issue(4); post(1);
#pragma unroll 1
  for (int j = 2; j < NS - 3; j++) {
    waitvm<2 * C::P>(); BAR();
    issue(j + 3); post(j);
  }
  waitvm<2 * C::P>(); BAR();
  ext(lds0 + (NS & 3) * C::STAGE); post(NS - 3);
  waitvm<C::P + XE>(); BAR();
  post(NS - 2);
  waitvm<XE>(); BAR();
  post(NS - 1);
  fin();
}
struct Nop { DI void operator()() const {} DI void operator()(int) const {} DI void operator()(unsigned) const {} };

template <class C> DI void make_wv(unsigned (&wv)[C::PW], unsigned w, unsigned lane, unsigned pitch) {
#pragma unroll
  for (int i = 0; i < C::PW; i++) {
    if (C::FRAGW) {
      unsigned pc = i * 4 + w, ks = pc / C::NB, nb = pc % C::NB;
      wv[i] = (ks * pitch + nb) * 1024 + lane * 16;
    } else {
      unsigned sidx = (i * 4 + w) * 64 + lane;
      constexpr unsigned SL = C::RB / 16;
      unsigned rho = sidx / SL, c = sidx % SL;
      unsigned k = C::XROW ? ((rho & ~31u) | (((rho >> 2) & 3) << 3) | (((rho >> 4) & 1) << 2) | (rho & 3)) : rho;
      wv[i] = k * pitch + ((c ^ wswz<C::RB>(rho)) << 4);
    }
  }
}
template <class C> DI void make_tokv(unsigned (&tv)[C::PT], unsigned w, unsigned lane, unsigned kstot) {
#pragma unroll
  for (int i = 0; i < C::PT; i++) {
    unsigned qd = i * 4 + w;
    if (C::XROW) { unsigned row = qd * 8 + (lane >> 3); tv[i] = row * 1280 + (((lane & 7) ^ ((row >> 1) & 7)) << 4); }
    else { unsigned mb = qd / C::KS, ks = qd % C::KS; tv[i] = (mb * kstot + ks) * 1024 + lane * 16; }
  }
}

template <class C, int NBW> struct Frags { bf16x8 t[C::MBW]; v4i16 wl[NBW], wh[NBW]; bf16x8 wf[NBW]; };
template <class C> struct CAddr { int tok[2]; int tokb[2]; int wa[4]; };
template <class C> DI void make_caddr(CAddr<C>& a, int wm, int wn, int lane, int pi = 0, int pj = 0) {
  const int g = lane >> 4, q = (lane >> 2) & 3, p = lane & 3, t = lane & 15;
  if (C::XROW) {
    a.tok[0] = (wm * C::MBW * 16 + t) * 128 + ((g ^ ((t >> 1) & 7)) << 4);
    a.tok[1] = (wm * C::MBW * 16 + t) * 128 + (((4 + g) ^ ((t >> 1) & 7)) << 4);
  } else { a.tok[0] = wm * C::MBW * C::KS * 1024 + lane * 16; a.tok[1] = 0; }
  {
    const int rot = pi * (C::XROW ? 2048 : C::KS * 1024);
    a.tokb[0] = a.tok[0] - rot; a.tokb[1] = a.tok[1] - rot; a.tok[0] += rot; a.tok[1] += rot;
  }
  int r0 = 4 * g + q, base = C::TOKB + r0 * C::RB + (p >> 1) * 16 + (p & 1) * 8;
  if (C::FRAGW) { a.wa[0] = C::TOKB + wn * C::NB0 * 1024 + lane * 16; a.wa[1] = a.wa[2] = a.wa[3] = 0; }
  else if (C::RB == 256) {
#pragma unroll
    for (int j = 0; j < 4; j++) a.wa[j] = base + (((wn * 4 + ((j + pj) & 3)) ^ (r0 & 7)) << 5);
  } else {
    int b = g & 1, n0 = wn * C::NB0;
    a.wa[0] = base + ((n0 ^ b) << 5);
    a.wa[1] = base + (((n0 + 1) ^ b) << 5) - 32;
    a.wa[2] = a.wa[3] = 0;
  }
}
template <class C, int NBW> struct RD { static constexpr int WR = C::FRAGW ? 1 : 2, NR = C::MBW + WR * NBW; };
template <class C, int NBW>
DI void read_one(Frags<C, NBW>& f, LAS const char* slot, const CAddr<C>& a, int ks, int r) {
  constexpr int WR = RD<C, NBW>::WR;
  int ti = -1, wj = -1, h = 0;
  if (r == 0) ti = 0;
  else if (r <= WR) { wj = 0; h = r - 1; }
  else if (r < WR + C::MBW) ti = r - WR;
  else { int q = r - C::MBW; wj = q / WR; h = q % WR; }
  if (ti >= 0) {
    const bool hi = ti >= C::MBW / 2;
    f.t[ti] = C::XROW ? lds128(slot + (hi ? a.tokb[ks & 1] : a.tok[ks & 1]) + ti * 2048)
                      : lds128(slot + (hi ? a.tokb[0] : a.tok[0]) + (ti * C::KS + ks) * 1024);
  } else if (C::FRAGW) {
    f.wf[wj] = lds128(slot + a.wa[0] + (ks * C::NB + wj) * 1024);
  } else {
    LAS const char* pw = C::RB == 256 ? slot + a.wa[wj] : slot + a.wa[wj & 1] + wj * 32;
    if (h == 0) f.wl[wj] = ldstr(pw + ks * 32 * C::RB);
    else f.wh[wj] = ldstr(pw + (ks * 32 + 16) * C::RB);
  }
}
template <class C, int NBW> DI bf16x8 wfrag(const Frags<C, NBW>& f, int j) { return C::FRAGW ? f.wf[j] : cat(f.wl[j], f.wh[j]); }
template <class C, int NBW>
DI void load_all(Frags<C, NBW>& f, LAS const char* slot, const CAddr<C>& a, int ks) {
#pragma unroll
  for (int r = 0; r < RD<C, NBW>::NR; r++) read_one<C, NBW>(f, slot, a, ks, r);
}
template <int NCS_, int CSM_, int NTS_> struct Extra {
  static constexpr int NCS = NCS_, CSM = CSM_, NTS = NTS_;
  f32x4 cs[NCS_ ? NCS_ : 1], ts[NTS_ ? NTS_ : 1], tq[NTS_ ? NTS_ : 1];
};
template <class C, int NBW, bool SWAP, bool BARR, bool RDN, class E>
DI void kstep(f32x4 (&acc)[C::MBW][NBW], E& ex, const Frags<C, NBW>& cur, Frags<C, NBW>& nxt,
              LAS const char* nslot, const CAddr<C>& a, int nks, bf16x8 ones) {
  constexpr int MBW = C::MBW, NM = MBW * NBW, NR = RD<C, NBW>::NR;
  constexpr int F0 = BARR ? MBW : 0, NSL = NM - F0, PER = (NR + NSL - 1) / NSL;
#pragma unroll
  for (int m = 0; m < NM; m++) {
    const int j = m / MBW, i = m % MBW;
    bf16x8 wf = wfrag<C, NBW>(cur, j);
    acc[i][j] = SWAP ? mfma(wf, cur.t[i], acc[i][j]) : mfma(cur.t[i], wf, acc[i][j]);
    if (i == MBW - 1 && j < E::NCS) ex.cs[j] = E::CSM ? mfma(wf, ones, ex.cs[j]) : mfma(ones, wf, ex.cs[j]);
    if (j == 2 && i < E::NTS) ex.ts[i] = mfma(ones, cur.t[i], ex.ts[i]);
    if (j == 3 && i < E::NTS) ex.tq[i] = mfma(cur.t[i], cur.t[i], ex.tq[i]);
    if (BARR && m == F0 - 1) { SB(); BAR(); }
    if (RDN && m >= F0) {
#pragma unroll
      for (int r = (m - F0) * PER; r < (m - F0 + 1) * PER && r < NR; r++) read_one<C, NBW>(nxt, nslot, a, nks, r);
    }
    SB();
  }
}
template <class C, int NBW, bool SWAP, bool LAST, class E>
DI void consume(f32x4 (&acc)[C::MBW][NBW], E& ex, Frags<C, NBW>& fa, LAS const char* lds, const CAddr<C>& a, int s0, int s1, bf16x8 ones) {
  Frags<C, NBW> fb;
  const int e = LAST ? s1 - 1 : s1;
#pragma unroll 1
  for (int s = s0; s < e; s++) {
    LAS const char* slot = lds + (s & 3) * C::STAGE;
    LAS const char* nsl = lds + ((s + 1) & 3) * C::STAGE;
#pragma unroll
    for (int ks = 0; ks < C::KS; ks += 2) {
      kstep<C, NBW, SWAP, false, true>(acc, ex, fa, fb, slot, a, ks + 1, ones);
      if (ks + 2 < C::KS) kstep<C, NBW, SWAP, false, true>(acc, ex, fb, fa, slot, a, ks + 2, ones);
      else kstep<C, NBW, SWAP, true, true>(acc, ex, fb, fa, nsl, a, 0, ones);
    }
  }
  if (LAST) {
    LAS const char* slot = lds + (e & 3) * C::STAGE;
#pragma unroll
    for (int ks = 0; ks < C::KS; ks += 2) {
      kstep<C, NBW, SWAP, false, true>(acc, ex, fa, fb, slot, a, ks + 1, ones);
      if (ks + 2 < C::KS) kstep<C, NBW, SWAP, false, true>(acc, ex, fb, fa, slot, a, ks + 2, ones);
      else kstep<C, NBW, SWAP, false, false>(acc, ex, fb, fa, slot, a, 0, ones);
    }
  }
}
template <int A, int B> DI void zero(f32x4 (&acc)[A][B]) {
#pragma unroll
  for (int i = 0; i < A; i++)
#pragma unroll
    for (int j = 0; j < B; j++) acc[i][j] = f32x4{0.f, 0.f, 0.f, 0.f};
}
template <int A, int M, int B> DI void zero(Extra<A, M, B>& e) {
#pragma unroll
  for (int i = 0; i < (A ? A : 1); i++) e.cs[i] = f32x4{0.f, 0.f, 0.f, 0.f};
#pragma unroll
  for (int i = 0; i < (B ? B : 1); i++) e.ts[i] = e.tq[i] = f32x4{0.f, 0.f, 0.f, 0.f};
}
DI bf16x8 ones_frag() { u32x4 o = {0x3f803f80u, 0x3f803f80u, 0x3f803f80u, 0x3f803f80u}; return __builtin_bit_cast(bf16x8, o); }
#define CBAR() asm volatile("s_waitcnt lgkmcnt(0)\n\ts_barrier" ::: "memory")

extern __shared__ __attribute__((aligned(16))) char smem[];
#define AUXF ((LAS float*)(lds + LDS_AUX))
DI f32x4 ldsf4(LAS const float* p) { return *(LAS const f32x4*)p; }
DI float sel4(f32x4 v, int k) { float a = (k & 1) ? v[1] : v[0], b = (k & 1) ? v[3] : v[2]; return (k & 2) ? b : a; }

extern "C" __global__ __launch_bounds__(512) void k_phase1(const char* __restrict__ x, const char* __restrict__ wqkv, const char* __restrict__ w1, char* __restrict__ ws,
                                              const char* __restrict__ wo, const char* __restrict__ w2, const char* __restrict__ w3, const char* __restrict__ w4) {
  typedef CfgA1 C;
  const int b = blockIdx.x, xcd = b & 7, idx = b >> 3;
  const int rt = (xcd >> 1) * 4 + (idx & 3), ct = (xcd & 1) * 8 + (idx >> 2);
  const int wid = __builtin_amdgcn_readfirstlane(threadIdx.x >> 6), lane = threadIdx.x & 63;
  LAS char* lds = (LAS char*)smem;
  STAMP(0, 0);
  if (ct >= 15) return;
  if (wid >= 4) {
    const int w = wid - 4;
    unsigned tv[C::PT], wv[C::PW];
    const char* wb = ct < 10 ? wqkv + (640 + ct * 128) * 2 : w1 + (ct - 10) * 256;
    const int pitch = ct < 10 ? 3840 : 1280;
    make_tokv<C>(tv, w, lane, 0); make_wv<C>(wv, w, lane, pitch);
    float s[4] = {0.f, 0.f, 0.f, 0.f}, q[4] = {0.f, 0.f, 0.f, 0.f};
    LAS const char* my = lds + w * 1024 + lane * 16;
    auto post = [&](int j) {
      LAS const char* p = my + (j & 3) * C::STAGE;
#pragma unroll
      for (int i = 0; i < 4; i++) {
        u32x4 v = *(LAS const u32x4*)(p + i * 4096);
#pragma unroll
        for (int e = 0; e < 4; e++) { s[i] = dot2(v[e], 0x3f803f80u, s[i]); q[i] = dot2(v[e], v[e], q[i]); }
      }
    };
    auto fin = [&]() {
#pragma unroll
      for (int i = 0; i < 4; i++) {
#pragma unroll
        for (int m = 1; m < 8; m <<= 1) { s[i] += shx(s[i], m); q[i] += shx(q[i], m); }
      }
      const int i = lane & 3;
      const float sv = sel4(f32x4{s[0], s[1], s[2], s[3]}, i), qv = sel4(f32x4{q[0], q[1], q[2], q[3]}, i);
      const float m = sv * (1.0f / D_), var = fmaxf(qv * (1.0f / D_) - m * m, 0.f);
      const float rs = __builtin_amdgcn_rsqf(var + EPS);
      float r = rs;
      if (ct >= 10) r = rs * __builtin_amdgcn_rsqf(var * rs * rs + EPS);
      const int row = (i * 4 + w) * 8 + (lane >> 3);
      if ((lane & 7) < 4) {
        AUXF[row] = r; AUXF[128 + row] = r * m;
        if (ct == 0) ((f32x2*)(ws + OFF_ST))[rt * 128 + row] = f32x2{m, rs};
      }
      BAR();
    };
    const unsigned junk = (unsigned)(size_t)lds + LDS_AUX + 3584;
    auto pre = [&]() {
      const unsigned gid = min(b * 4 + w, 719);
      const char* p1 = gid < 320 ? x + gid * 8192 : (gid < 620 ? wqkv + (gid - 320) * 8192 : w1 + (gid - 620) * 8192);
      dma4(p1, lane * 128, junk);
      touch_code(idx * 4 + w, lane, junk);
    };
    loader<C, 1 + XC, 0>(w, (unsigned)(size_t)lds, x + (size_t)rt * 128 * 1280, 128, tv, wb, 64 * pitch, wv, 10, pre, post, Nop(), fin);
    waitvm<0>();
    STAMP(0, 1);
    return;
  }
  const int wm = wid >> 1, wn = wid & 1, g = lane >> 4, tk = lane & 15;
  CAddr<C> a; make_caddr<C>(a, wm, wn, lane, 0, 2 * wm);
  f32x4 acc[4][4]; zero(acc);
  Extra<2, 0, 0> ex; zero(ex);
  const bf16x8 ones = ones_frag();
  Frags<C, 4> fa;
  CBAR(); STAMP(0, 1); CSTAMP(0, 6); load_all<C, 4>(fa, lds, a, 0);
  if (ct < 10) consume<C, 4, false, true>(acc, ex, fa, lds, a, 0, 10, ones);
  else consume<C, 4, true, true>(acc, ex, fa, lds, a, 0, 10, ones);
  STAMP(0, 2); CSTAMP(0, 7);
  {
    const unsigned gid = b * 4 + wid;
    const char* p2 = gid < 100 ? wo + gid * 8192 : (gid < 200 ? w2 + (gid - 100) * 8192 : (gid < 440 ? w3 + (gid - 200) * 8192 : w4 + (gid - 440) * 8192));
    if (gid < 680) dma4(p2, lane * 128, (unsigned)(size_t)lds + LDS_AUX + 3584);
  }
  if (g == 0) {
#pragma unroll
    for (int i = 0; i < 2; i++) AUXF[256 + wn * 64 + (2 * wm + i) * 16 + tk] = ex.cs[i][0];
  }
  CBAR();
  STAMP(0, 3);
  if (ct < 10) {
    char* dst = ws + (ct < 5 ? OFF_PK : OFF_V) + lane * 16;
    const int cb0 = (ct < 5 ? ct : ct - 5) * 8 + wn * 4;
    float cs[4]; f32x2 zs[4];
#pragma unroll
    for (int j = 0; j < 4; j++) { cs[j] = AUXF[256 + wn * 64 + ((j + 2 * wm) & 3) * 16 + tk]; zs[j] = f32x2{0.f, 0.f}; }
#pragma unroll
    for (int ip = 0; ip < 2; ip++) {
      const int tb = wm * 64 + ip * 32 + 4 * g;
      f32x4 r0 = ldsf4(AUXF + tb), r1 = ldsf4(AUXF + tb + 16), m0 = ldsf4(AUXF + 128 + tb), m1 = ldsf4(AUXF + 128 + tb + 16);
      char* d2 = dst + (size_t)((rt * 4 + wm * 2 + ip) * 40 + cb0) * 1024;
#pragma unroll
      for (int j = 0; j < 4; j++) {
        f32x4 t0 = r0 * acc[2 * ip][j] - m0 * cs[j], t1 = r1 * acc[2 * ip + 1][j] - m1 * cs[j];
        u32x4 o;
        if (ct < 5) {
          o = u32x4{pk2v(phi2(lo2(t0))), pk2v(phi2(hi2(t0))), pk2v(phi2(lo2(t1))), pk2v(phi2(hi2(t1)))};
          zs[j] += (unpk(o[0]) + unpk(o[1])) + (unpk(o[2]) + unpk(o[3]));
        } else {
          o = u32x4{pk2v(lo2(t0)), pk2v(hi2(t0)), pk2v(lo2(t1)), pk2v(hi2(t1))};
        }
        st16(d2 + ((j + 2 * wm) & 3) * 1024, o);
      }
    }
    if (ct < 5) {
      float z[4];
#pragma unroll
      for (int j = 0; j < 4; j++) { z[j] = zs[j][0] + zs[j][1]; z[j] += shx(z[j], 16); z[j] += shx(z[j], 32); }
      if (wm == 1 && lane < 16) {
#pragma unroll
        for (int j = 0; j < 4; j++) AUXF[704 + wn * 64 + ((j + 2) & 3) * 16 + lane] = z[j];
      }
      CBAR();
      if (wm == 0 && lane < 16) {
        float* zp = (float*)(ws + OFF_ZP) + rt * 640 + ct * 128 + wn * 64;
#pragma unroll
        for (int j = 0; j < 4; j++) zp[j * 16 + lane] = z[j] + AUXF[704 + wn * 64 + j * 16 + lane];
      }
    }
  } else {
    const int c0 = (ct - 10) * 128 + wn * 64;
#pragma unroll
    for (int i = 0; i < 4; i++) {
      const int row = wm * 64 + i * 16 + tk;
      const float rr = AUXF[row], rm = AUXF[128 + row];
#pragma unroll
      for (int jp = 0; jp < 2; jp++) {
        const int jt = jp ^ wm;
        u32x4 o;
#pragma unroll
        for (int h = 0; h < 2; h++) {
          f32x4 cs = ldsf4(AUXF + 256 + wn * 64 + (2 * jt + h) * 16 + 4 * g);
          f32x4 t = rr * acc[i][2 * jp + h] - rm * cs;
          o[2 * h] = pk2v(gelu2(lo2(t))); o[2 * h + 1] = pk2v(gelu2(hi2(t)));
        }
        const int col = c0 + jt * 32, grp = col / 160;
        const int dl = grp == 0 ? 1 : (grp == 1 ? -1 : (grp == 2 ? 2 : -2));
        int td = rt * 128 + row + dl;
        const bool ok = td >= 0 && td < S_;
        td &= S_ - 1;
        if (!ok) o = u32x4{0, 0, 0, 0};
        st16(ws + OFF_H + ((size_t)((td >> 4) * 20 + (col >> 5)) * 64 + g * 16 + (td & 15)) * 16, o);
      }
    }
  }
  waitvm<0>();
  STAMPEND(0);
}

extern "C" __global__ __launch_bounds__(512) void k_phase2(const char* __restrict__ x, const char* __restrict__ wqkv, const char* __restrict__ wo, const char* __restrict__ w2,
                                              const char* __restrict__ w4, char* __restrict__ ws) {
  const int b = blockIdx.x;
  const int wid = __builtin_amdgcn_readfirstlane(threadIdx.x >> 6), lane = threadIdx.x & 63;
  LAS char* lds = (LAS char*)smem;
  const int g = lane >> 4, tk = lane & 15;
  STAMP(1, 0);
  if (b < 128) {
    typedef CfgQ C;
    const int rt = b >> 2, ct = b & 3;
    if (wid >= 4) {
      const int w = wid - 4;
      unsigned tv[C::PT], wv[C::PW];
      make_tokv<C>(tv, w, lane, 0); make_wv<C>(wv, w, lane, 3840);
      loader<C, XC, 0>(w, (unsigned)(size_t)lds, x + (size_t)rt * 64 * 1280, 128, tv, wqkv + ct * 320, 64 * 3840, wv, 10, [&]() { touch_code((b >> 3) * 4 + w, lane, (unsigned)(size_t)lds + LDS_AUX + 3584); }, Nop(), Nop(), Nop());
      STAMP(1, 1);
      return;
    }
    const int wm = wid >> 1, wn = wid & 1;
    float zp[16];
    const int t = threadIdx.x;
    if (t < 160) {
      const float* p = (const float*)(ws + OFF_ZP) + ct * 160 + t;
#pragma unroll
      for (int r = 0; r < 16; r++) zp[r] = p[r * 640];
    }
    f32x2 st[2];
#pragma unroll
    for (int i = 0; i < 2; i++) st[i] = ((const f32x2*)(ws + OFF_ST))[rt * 64 + wm * 32 + i * 16 + tk];
    CAddr<C> a; make_caddr<C>(a, wm, wn, lane);
    f32x4 acc[2][5]; zero(acc);
    Extra<5, 1, 0> ex; zero(ex);
    const bf16x8 ones = ones_frag();
    Frags<C, 5> fa;
    CBAR(); STAMP(1, 1); CSTAMP(1, 6); load_all<C, 5>(fa, lds, a, 0);
    if (t < 160) {
      float z = 0.f;
#pragma unroll
      for (int r = 0; r < 16; r += 4) z += (zp[r] + zp[r + 1]) + (zp[r + 2] + zp[r + 3]);
      AUXF[512 + t] = z;
    }
    consume<C, 5, true, true>(acc, ex, fa, lds, a, 0, 10, ones);
    STAMP(1, 2); CSTAMP(1, 7);
#pragma unroll
    for (int i = 0; i < 2; i++) {
      const float rs = st[i][1], rm = rs * st[i][0];
      f32x4 pq[5]; float part[5];
#pragma unroll
      for (int j = 0; j < 5; j++) {
        f32x4 z = ldsf4(AUXF + 512 + wn * 80 + j * 16 + 4 * g);
        f32x4 tt = rs * acc[i][j] - rm * ex.cs[j];
        f32x2 p0 = phi2(lo2(tt)), p1 = phi2(hi2(tt));
        pq[j] = f32x4{p0[0], p0[1], p1[0], p1[1]};
        f32x2 s = p0 * lo2(z) + p1 * hi2(z);
        part[j] = s[0] + s[1];
      }
      float dh[4];
      dh[0] = part[0] + (g == 0 ? part[1] : 0.f);
      dh[1] = (g >= 1 ? part[1] : 0.f) + (g <= 1 ? part[2] : 0.f);
      dh[2] = (g >= 2 ? part[2] : 0.f) + (g <= 2 ? part[3] : 0.f);
      dh[3] = (g == 3 ? part[3] : 0.f) + part[4];
#pragma unroll
      for (int h = 0; h < 4; h++) { dh[h] += shx(dh[h], 16); dh[h] += shx(dh[h], 32); dh[h] = rcp(dh[h] + 1e-6f); }
      const float sc[5] = {dh[0], g == 0 ? dh[0] : dh[1], g <= 1 ? dh[1] : dh[2], g <= 2 ? dh[2] : dh[3], dh[3]};
#pragma unroll
      for (int j = 0; j < 5; j++) pq[j] *= sc[j];
      char* dst = ws + OFF_PQ + ((size_t)(rt * 4 + wm * 2 + i) * 20 + ct * 5) * 1024 + lane * 16;
      st16(dst + (3 * wn) * 1024, u32x4{pk2v(lo2(pq[0])), pk2v(hi2(pq[0])), pk2v(lo2(pq[1])), pk2v(hi2(pq[1]))});
      st16(dst + (3 * wn + 1) * 1024, u32x4{pk2v(lo2(pq[2])), pk2v(hi2(pq[2])), pk2v(lo2(pq[3])), pk2v(hi2(pq[3]))});
      *(u32x2*)(dst + 2 * 1024 + wn * 8) = u32x2{pk2v(lo2(pq[4])), pk2v(hi2(pq[4]))};
    }
    STAMPEND(1);
  } else if (b < 208) {
    typedef CfgA C;
    const int bb = b - 128, rt = (bb & 7) * 2 + (bb >> 3) / 5, ct = (bb >> 3) % 5;
    if (wid >= 4) {
      const int w = wid - 4;
      unsigned tv[C::PT], wv[C::PW];
      make_tokv<C>(tv, w, lane, 20); make_wv<C>(wv, w, lane, 1280);
      const char* xb = x + (size_t)rt * 128 * 1280 + ct * 256;
      const unsigned xr = w * 4 + g, xo = xr * 1280 + ((tk ^ xr) << 4);
      auto ext = [&](unsigned dst) {
#pragma unroll
        for (int i = 0; i < 8; i++) dma16(xb, xo + i * 16 * 1280, dst + (i * 4 + w) * 1024);
      };
      auto fin = [&]() { waitvm<0>(); BAR(); };
      loader<C, 0, 8>(w, (unsigned)(size_t)lds, ws + OFF_H + (size_t)rt * 8 * 20 * 1024, 2048, tv, w2 + ct * 256, 64 * 1280, wv, 10, Nop(), Nop(), ext, fin);
      STAMP(1, 1);
      return;
    }
    const int wm = wid >> 1, wn = wid & 1;
    f32x2 st[4];
#pragma unroll
    for (int i = 0; i < 4; i++) st[i] = ((const f32x2*)(ws + OFF_ST))[rt * 128 + wm * 64 + i * 16 + tk];
    CAddr<C> a; make_caddr<C>(a, wm, wn, lane);
    f32x4 acc[4][4]; zero(acc);
    Extra<0, 0, 0> ex;
    const bf16x8 ones = ones_frag();
    Frags<C, 4> fa;
    CBAR(); STAMP(1, 1); CSTAMP(1, 6); load_all<C, 4>(fa, lds, a, 0);
    consume<C, 4, true, true>(acc, ex, fa, lds, a, 0, 10, ones);
    STAMP(1, 2); CSTAMP(1, 7);
    CBAR();
    LAS const char* xs = lds + 2 * C::STAGE + (wm * 64 + tk) * 256 + (g & 1) * 8;
#pragma unroll
    for (int i = 0; i < 4; i++) {
      const float rs = st[i][1], rm = st[i][0] * rs;
      f32x2 s1 = {0.f, 0.f}, s2 = {0.f, 0.f};
      char* dst = ws + OFF_X1 + ((size_t)(rt * 8 + wm * 4 + i) * 20 + ct * 4 + wn * 2) * 1024 + lane * 16;
#pragma unroll
      for (int jp = 0; jp < 2; jp++) {
        u32x4 o;
#pragma unroll
        for (int h = 0; h < 2; h++) {
          u32x2 xx = *(LAS const u32x2*)(xs + i * 16 * 256 + (((wn * 8 + (2 * jp + h) * 2 + (g >> 1)) ^ tk) << 4));
          f32x4 c = acc[i][2 * jp + h] - rm;
          o[2 * h] = pk2v(unpk(xx[0]) * rs + lo2(c));
          o[2 * h + 1] = pk2v(unpk(xx[1]) * rs + hi2(c));
        }
#pragma unroll
        for (int e = 0; e < 4; e++) { f32x2 v = unpk(o[e]); s1 += v; s2 += v * v; }
        st16(dst + jp * 1024, o);
      }
      float a1 = s1[0] + s1[1], a2 = s2[0] + s2[1];
      a1 += shx(a1, 16); a2 += shx(a2, 16); a1 += shx(a1, 32); a2 += shx(a2, 32);
      if (g == 0) ((f32x2*)(ws + OFF_XS))[(ct * 2 + wn) * 2048 + rt * 128 + wm * 64 + i * 16 + tk] = f32x2{a1, a2};
    }
    STAMPEND(1);
  } else if (b < 240) {
    const int h = b - 208, hh = h & 3, cb0 = 5 * (h >> 2) + hh;
    LAS char* wt = lds + 40960;
    {
      const char* wb = wo + (size_t)cb0 * 16 * 1280;
#pragma unroll
      for (int i = 0; i < 5; i++) {
        const unsigned pc = i * 8 + wid, s = pc * 64 + lane, qt = s / 640, r = (s % 640) / 20, c = s % 20;
        dma16(wb, r * 1280 + qt * 320 + ((c ^ (2 * ((r >> 2) & 1))) << 4), (unsigned)(size_t)wt + pc * 1024);
      }
    }
    bf16x8 pk[8][2], vv[8][2];
#pragma unroll
    for (int u = 0; u < 8; u++)
#pragma unroll
      for (int j = 0; j < 2; j++) {
        const size_t o = ((size_t)(wid * 8 + u) * 40 + cb0 + j) * 1024 + lane * 16;
        pk[u][j] = *(const bf16x8*)(ws + OFF_PK + o); vv[u][j] = *(const bf16x8*)(ws + OFF_V + o);
      }
    f32x4 acc[4];
#pragma unroll
    for (int i = 0; i < 4; i++) acc[i] = f32x4{0.f, 0.f, 0.f, 0.f};
#pragma unroll
    for (int u = 0; u < 8; u++)
#pragma unroll
      for (int i = 0; i < 4; i++) acc[i] = mfma(vv[u][i & 1], pk[u][i >> 1], acc[i]);
    STAMP(1, 2);
#pragma unroll
    for (int i = 0; i < 4; i++) *(LAS f32x4*)(lds + (wid * 4 + i) * 1024 + lane * 16) = acc[i];
    waitvm<0>();
    BAR();
    if (wid < 4) {
      f32x4 s = *(LAS const f32x4*)(lds + wid * 1024 + lane * 16);
#pragma unroll
      for (int w = 1; w < 8; w++) s += *(LAS const f32x4*)(lds + (w * 4 + wid) * 1024 + lane * 16);
      const unsigned ee = 4 * (wid & 1) + g - hh;
      u32x2 o = u32x2{pk2v(lo2(s)), pk2v(hi2(s))};
      if (ee >= 5u) o = u32x2{0u, 0u};
      *(LAS u32x2*)(lds + 32768 + wid * 512 + lane * 8) = o;
    }
    BAR();
    STAMP(1, 3);
    {
      const int q = (lane >> 2) & 3, p = lane & 3;
      bf16x8 af[2]; char* dst[2]; bool ok[2];
#pragma unroll
      for (int db = 0; db < 2; db++) {
        const u32x2 h0 = *(LAS const u32x2*)(lds + 32768 + (db * 2) * 512 + lane * 8), h1 = *(LAS const u32x2*)(lds + 32768 + (db * 2 + 1) * 512 + lane * 8);
        af[db] = __builtin_bit_cast(bf16x8, u32x4{h0[0], h0[1], h1[0], h1[1]});
        const int beta = cb0 + db, GG = beta / 10, j = beta % 10, wn = j / 5, jj = j % 5;
        const int kap = jj == 4 ? 5 * GG + 2 : 5 * GG + 3 * wn + (jj >> 1), tt = jj == 4 ? wn : (jj & 1);
        dst[db] = ws + OFF_ME + (size_t)kap * 40 * 1024 + lane * 16 + tt * 8;
        ok[db] = (unsigned)(4 * db + g - hh) < 5u;
      }
#pragma unroll
      for (int i = 0; i < 5; i++) {
        const int nb = wid * 5 + i, qt = nb / 10, nl = nb % 10;
        LAS const char* src = wt + qt * 10240 + (4 * g + q) * 320 + ((nl ^ (g & 1)) << 5) + p * 8;
        const bf16x8 wf = cat(ldstr(src), ldstr(src + 16 * 320));
#pragma unroll
        for (int db = 0; db < 2; db++) {
          const f32x4 c = mfma(af[db], wf, f32x4{0.f, 0.f, 0.f, 0.f});
          if (ok[db]) *(u32x2*)(dst[db] + nb * 1024) = u32x2{pk2v(lo2(c)), pk2v(hi2(c))};
        }
      }
    }
    STAMPEND(1);
  } else {
    LAS char* base = lds + wid * 16384;
    const unsigned lb = (unsigned)(size_t)base;
    const int q = (lane >> 2) & 3, p = lane & 3, r0 = 4 * g + q;
    const int u = (b - 240) * 8 + wid;
    if (u < 120) {
      const int kb = u / 5, cb = u % 5;
      const char* src = w4 + kb * 64 * 1280 + cb * 256;
      char* dst = ws + OFF_W4 + (kb * 80 + cb * 8) * 1024 + lane * 16;
#pragma unroll
      for (int i = 0; i < 16; i++) {
        const unsigned rho = i * 4 + g;
        dma16(src, rho * 1280 + (((lane & 15) ^ wswz<256>(rho)) << 4), lb + i * 1024);
      }
      waitvm<0>();
      LAS const char* pw = base + r0 * 256 + p * 8;
#pragma unroll
      for (int nb = 0; nb < 8; nb++)
#pragma unroll
        for (int ks = 0; ks < 2; ks++) {
          LAS const char* pp = pw + ((nb ^ (r0 & 7)) << 5) + ks * 32 * 256;
          st16(dst + (ks * 40 + nb) * 1024, __builtin_bit_cast(u32x4, cat(ldstr(pp), ldstr(pp + 16 * 256))));
        }
    }
    STAMPEND(1);
  }
}

extern "C" __global__ __launch_bounds__(512) void k_phase3(const char* __restrict__ w3, char* __restrict__ ws) {
  const int b = blockIdx.x, xcd = b & 7;
  const int wid = __builtin_amdgcn_readfirstlane(threadIdx.x >> 6), lane = threadIdx.x & 63;
  LAS char* lds = (LAS char*)smem;
  const int wm = (wid >> 1) & 1, wn = wid & 1, g = lane >> 4, tk = lane & 15;
  STAMP(2, 0);
  if (b < 192) {
    typedef CfgA C;
    const int idx = b >> 3;
    const int rt = (xcd >> 1) * 4 + (idx & 3), ct = (xcd & 1) * 6 + (idx >> 2);
    if (wid >= 4) {
      const int w = wid - 4;
      unsigned tv[C::PT], wv[C::PW];
      make_tokv<C>(tv, w, lane, 20); make_wv<C>(wv, w, lane, 3072);
      loader<C, XC, 0>(w, (unsigned)(size_t)lds, ws + OFF_X1 + (size_t)rt * 8 * 20 * 1024, 2048, tv, w3 + ct * 256, 64 * 3072, wv, 10, [&]() { touch_code((b >> 3) * 4 + w, lane, (unsigned)(size_t)lds + LDS_AUX + 3584); }, Nop(), Nop(), Nop());
      STAMP(2, 1);
      return;
    }
    f32x2 xs[10];
    if (wid < 2) {
      const f32x2* p = (const f32x2*)(ws + OFF_XS) + rt * 128 + threadIdx.x;
#pragma unroll
      for (int i = 0; i < 10; i++) xs[i] = p[i * 2048];
    }
    CAddr<C> a; make_caddr<C>(a, wm, wn, lane);
    f32x4 acc[4][4]; zero(acc);
    Extra<4, 1, 0> ex; zero(ex);
    const bf16x8 ones = ones_frag();
    Frags<C, 4> fa;
    CBAR(); STAMP(2, 1); CSTAMP(2, 6); load_all<C, 4>(fa, lds, a, 0);
    if (wid < 2) {
      f32x2 s = ((xs[0] + xs[1]) + (xs[2] + xs[3])) + ((xs[4] + xs[5]) + (xs[6] + xs[7])) + (xs[8] + xs[9]);
      float m = s[0] * (1.0f / D_), var = fmaxf(s[1] * (1.0f / D_) - m * m, 0.f);
      float rs = __builtin_amdgcn_rsqf(var + EPS);
      AUXF[threadIdx.x] = rs; AUXF[128 + threadIdx.x] = rs * m;
    }
    consume<C, 4, true, true>(acc, ex, fa, lds, a, 0, 10, ones);
    STAMP(2, 2); CSTAMP(2, 7);
#pragma unroll
    for (int i = 0; i < 4; i++) {
      const int row = wm * 64 + i * 16 + tk;
      const float rs = AUXF[row], rm = AUXF[128 + row];
      char* dst = ws + OFF_H3 + ((size_t)(rt * 8 + wm * 4 + i) * 48 + ct * 4 + wn * 2) * 1024 + lane * 16;
#pragma unroll
      for (int jp = 0; jp < 2; jp++) {
        u32x4 o;
#pragma unroll
        for (int h = 0; h < 2; h++) {
          f32x4 t = rs * acc[i][2 * jp + h] - rm * ex.cs[2 * jp + h];
          o[2 * h] = pk2v(gelu2(lo2(t))); o[2 * h + 1] = pk2v(gelu2(hi2(t)));
        }
        st16(dst + jp * 1024, o);
      }
    }
    STAMPEND(2);
  } else {
    typedef CfgS C;
    const int idx = (b - 192) >> 3;
    const int rt = (xcd >> 1) * 4 + (idx & 3), ct = (xcd & 1) * 2 + (idx >> 2);
    if (wid >= 4) {
      const int w = wid - 4;
      unsigned tv[C::PT], wv[C::PW];
      make_tokv<C>(tv, w, lane, 20); make_wv<C>(wv, w, lane, 40);
      loader<C, 0, 0>(w, (unsigned)(size_t)lds, ws + OFF_PQ + (size_t)rt * 8 * 20 * 1024, 2048, tv, ws + OFF_ME + ct * 10 * 1024, 80 * 1024, wv, 10, Nop(), Nop(), Nop(), Nop());
      STAMP(2, 1);
      return;
    }
    CAddr<C> a; make_caddr<C>(a, wm, wn, lane);
    f32x4 acc[4][5]; zero(acc);
    Extra<0, 0, 0> ex;
    const bf16x8 ones = ones_frag();
    Frags<C, 5> fa;
    CBAR(); STAMP(2, 1); CSTAMP(2, 6); load_all<C, 5>(fa, lds, a, 0);
    consume<C, 5, true, true>(acc, ex, fa, lds, a, 0, 10, ones);
    STAMP(2, 2); CSTAMP(2, 7);
#pragma unroll
    for (int i = 0; i < 4; i++) {
      char* dst = ws + OFF_SA + ((size_t)(rt * 8 + wm * 4 + i) * 20 + ct * 5) * 1024 + lane * 16;
      u32x2 o[5];
#pragma unroll
      for (int j = 0; j < 5; j++) o[j] = u32x2{pk2v(lo2(acc[i][j])), pk2v(hi2(acc[i][j]))};
      if (wn == 0) {
        st16(dst, u32x4{o[0][0], o[0][1], o[1][0], o[1][1]});
        st16(dst + 1024, u32x4{o[2][0], o[2][1], o[3][0], o[3][1]});
        *(u32x2*)(dst + 2048) = o[4];
      } else {
        *(u32x2*)(dst + 2048 + 8) = o[0];
        st16(dst + 3072, u32x4{o[1][0], o[1][1], o[2][0], o[2][1]});
        st16(dst + 4096, u32x4{o[3][0], o[3][1], o[4][0], o[4][1]});
      }
    }
    STAMPEND(2);
  }
}

template <int NBW>
DI void p4_consumer(char* __restrict__ out, char* __restrict__ ws, LAS char* lds, int rt, int ct, int wm, int wn, int lane) {
  typedef CfgC C;
  const int g = lane >> 4, tk = lane & 15;
  u32x2 sav[2][NBW], x1v[2][NBW];
  CAddr<C> a; make_caddr<C>(a, wm, wn, lane);
  f32x4 h2[2][NBW]; zero(h2);
  Extra<0, 0, 0> ex;
  const bf16x8 ones = ones_frag();
  Frags<C, NBW> fa;
  CBAR(); STAMP(3, 1); CSTAMP(3, 6); load_all<C, NBW>(fa, lds, a, 0);
  consume<C, NBW, true, false>(h2, ex, fa, lds, a, 0, 9, ones);
#pragma unroll
  for (int i = 0; i < 2; i++)
#pragma unroll
    for (int j = 0; j < NBW; j++) {
      const int blk = ct * 5 + wn * 3 + j;
      const size_t o = ((size_t)(rt * 4 + wm * 2 + i) * 20 + (blk >> 1)) * 1024 + lane * 16 + (blk & 1) * 8;
      x1v[i][j] = *(const u32x2*)(ws + OFF_X1 + o);
      sav[i][j] = *(const u32x2*)(ws + OFF_SA + o);
    }
  consume<C, NBW, true, true>(h2, ex, fa, lds, a, 9, 12, ones);
  STAMP(3, 2); CSTAMP(3, 7);
  CBAR();
  LAS char* xs = lds + (wm * 32 + tk) * 160 + wn * 96 + g * 8;
#pragma unroll
  for (int i = 0; i < 2; i++)
#pragma unroll
    for (int j = 0; j < NBW; j++) {
      LAS u32x2* px = (LAS u32x2*)(xs + i * 16 * 160 + j * 32);
      const u32x2 xv = *px;
      u32x2 o;
#pragma unroll
      for (int e = 0; e < 2; e++) {
        f32x2 bb = unpk(x1v[i][j][e]) + (e ? hi2(h2[i][j]) : lo2(h2[i][j]));
        f32x2 z = bb * -1.4426950408889634f;
        f32x2 d = f32x2{ex2(z[0]), ex2(z[1])} + 1.0f;
        f32x2 sl = bb * f32x2{rcp(d[0]), rcp(d[1])};
        o[e] = pk2v(unpk(xv[e]) + unpk(sav[i][j][e]) * sl);
      }
      *px = o;
    }
  CBAR();
  const int t = (wm * 2 + wn) * 64 + lane;
#pragma unroll
  for (int i = 0; i < 3; i++) {
    const int ch = t + 256 * i;
    if (ch < 640) {
      const int row = ch / 10, c = ch % 10;
      *(u32x4*)(out + (size_t)(rt * 64 + row) * 1280 + ct * 160 + c * 16) = *(LAS const u32x4*)(lds + ch * 16);
    }
  }
  STAMPEND(3);
}
extern "C" __global__ __launch_bounds__(512) void k_phase4(const char* __restrict__ x, char* __restrict__ ws, char* __restrict__ out) {
  typedef CfgC C;
  const int b = blockIdx.x, xcd = b & 7, idx = b >> 3;
  const int rt = (xcd >> 1) * 8 + (idx & 7), ct = (xcd & 1) * 4 + (idx >> 3);
  const int wid = __builtin_amdgcn_readfirstlane(threadIdx.x >> 6), lane = threadIdx.x & 63;
  LAS char* lds = (LAS char*)smem;
  STAMP(3, 0);
  if (wid >= 4) {
    const int w = wid - 4;
    unsigned tv[C::PT], wv[C::PW];
    make_tokv<C>(tv, w, lane, 48); make_wv<C>(wv, w, lane, 40);
    const char* xb = x + (size_t)rt * 64 * 1280 + ct * 160;
    auto ext = [&](unsigned dst) {
#pragma unroll
      for (int i = 0; i < 3; i++) {
        const unsigned ch = min((i * 4 + w) * 64 + lane, 639);
        dma16(xb, (ch / 10) * 1280 + (ch % 10) * 16, dst + (i * 4 + w) * 1024);
      }
    };
    auto fin = [&]() { waitvm<0>(); BAR(); };
    loader<C, XC, 3>(w, (unsigned)(size_t)lds, ws + OFF_H3 + (size_t)rt * 4 * 48 * 1024, 4096, tv, ws + OFF_W4 + ct * 5 * 1024, 4 * 40 * 1024, wv, 12, [&]() { touch_code((b >> 3) * 4 + w, lane, (unsigned)(size_t)lds + LDS_AUX + 3584); }, Nop(), ext, fin);
    STAMP(3, 1);
    return;
  }
  const int wm = wid >> 1, wn = wid & 1;
  if (wn == 0) p4_consumer<3>(out, ws, lds, rt, ct, wm, wn, lane);
  else p4_consumer<2>(out, ws, lds, rt, ct, wm, wn, lane);
}

extern "C" void kernel_launch(void* const* d_in, const int* in_sizes, int n_in, void* d_out, int out_size, void* d_ws, size_t ws_size, hipStream_t stream) {
  const char* x = (const char*)d_in[0]; const char* wqkv = (const char*)d_in[1]; const char* wo = (const char*)d_in[2];
  const char* w1 = (const char*)d_in[4]; const char* w2 = (const char*)d_in[5]; const char* w3 = (const char*)d_in[6]; const char* w4 = (const char*)d_in[7];
  char* ws = (char*)d_ws;
  (void)hipFuncSetAttribute((const void*)k_phase1, hipFuncAttributeMaxDynamicSharedMemorySize, LDS_TOTAL);
  (void)hipFuncSetAttribute((const void*)k_phase2, hipFuncAttributeMaxDynamicSharedMemorySize, LDS_TOTAL);
  (void)hipFuncSetAttribute((const void*)k_phase3, hipFuncAttributeMaxDynamicSharedMemorySize, LDS_TOTAL);
  (void)hipFuncSetAttribute((const void*)k_phase4, hipFuncAttributeMaxDynamicSharedMemorySize, LDS_TOTAL);
  k_phase1<<<256, 512, LDS_TOTAL, stream>>>(x, wqkv, w1, ws, wo, w2, w3, w4);
  k_phase2<<<256, 512, LDS_TOTAL, stream>>>(x, wqkv, wo, w2, w4, ws);
  k_phase3<<<256, 512, LDS_TOTAL, stream>>>(w3, ws);
  k_phase4<<<256, 512, LDS_TOTAL, stream>>>(x, ws, (char*)d_out);
}
```

```cpp
#include <hip/hip_runtime.h>
#include <stdint.h>

typedef __attribute__((ext_vector_type(8))) __bf16 bf16x8;
typedef __attribute__((ext_vector_type(2))) __bf16 bf16x2;
typedef __attribute__((ext_vector_type(4))) float f32x4;
typedef __attribute__((ext_vector_type(2))) float f32x2;
typedef __attribute__((ext_vector_type(4))) short v4i16;
typedef __attribute__((ext_vector_type(8))) short v8i16;
typedef __attribute__((ext_vector_type(4))) unsigned u32x4;
typedef __attribute__((ext_vector_type(2))) unsigned u32x2;
#define LAS __attribute__((address_space(3)))
#define DI __device__ __forceinline__
#define SB() __builtin_amdgcn_sched_barrier(0)

constexpr int S_ = 2048, D_ = 640, F_ = 1536;
constexpr size_t KB = 1024;
constexpr size_t OFF_PK = 0;
constexpr size_t OFF_V = OFF_PK + 64 * 40 * KB;
constexpr size_t OFF_H = OFF_V + 64 * 40 * KB;
constexpr size_t OFF_X1 = OFF_H + 128 * 20 * KB;
constexpr size_t OFF_H3 = OFF_X1 + 128 * 20 * KB;
constexpr size_t OFF_PQ = OFF_H3 + 128 * 48 * KB;
constexpr size_t OFF_SA = OFF_PQ + 128 * 20 * KB;
constexpr size_t OFF_W4 = OFF_SA + 128 * 20 * KB;
constexpr size_t OFF_ME = OFF_W4 + 48 * 40 * KB;
constexpr size_t OFF_ZP = OFF_ME + 20 * 40 * KB;
constexpr size_t OFF_ST = OFF_ZP + 16 * 640 * 4;
constexpr size_t OFF_XS = OFF_ST + 2048 * 8;
constexpr float EPS = 1e-5f;
#define STAMP(k, i)
#define STAMPEND(k)
#define CSTAMP(k, i)

DI void dma16(const void* sbase, unsigned voff, unsigned ldsdst) {
  unsigned keep;
  asm volatile("s_mov_b32 %0, m0\n\ts_mov_b32 m0, %3\n\ts_nop 0\n\tglobal_load_lds_dwordx4 %1, %2\n\ts_mov_b32 m0, %0"
               : "=&s"(keep) : "v"(voff), "s"(sbase), "s"(ldsdst) : "memory");
}
DI void dma4(const void* sbase, unsigned voff, unsigned ldsdst) {
  unsigned keep;
  asm volatile("s_mov_b32 %0, m0\n\ts_mov_b32 m0, %3\n\ts_nop 0\n\tglobal_load_lds_dword %1, %2\n\ts_mov_b32 m0, %0"
               : "=&s"(keep) : "v"(voff), "s"(sbase), "s"(ldsdst) : "memory");
}
#define CODE_ADDR(sym, out) asm volatile("s_getpc_b64 s[40:41]\n\ts_add_u32 s40, s40, " sym "@rel32@lo+4\n\ts_addc_u32 s41, s41, " sym "@rel32@hi+12\n\ts_mov_b64 %0, s[40:41]" \
                                         : "=s"(out) :: "s40", "s41", "scc")
#define L2T 0
constexpr unsigned P4LEN = 9088;
constexpr int XC = 1;
DI void touch_code(unsigned gid, unsigned lane, unsigned ldsdst) {
  constexpr int dummy = 0; (void)dummy;
  unsigned long long a, b;
  CODE_ADDR("k_phase1", a); CODE_ADDR("k_phase4", b);
  const unsigned len = min((unsigned)(b - a) + P4LEN, 131072u), o = (gid * 64 + lane) * 128;
  dma4((const char*)a, min(o, len - 128), ldsdst);
}
DI void st16(void* p, u32x4 v) { *(u32x4*)p = v; }
DI void st16wt(void* p, u32x4 v) { asm volatile("global_store_dwordx4 %0, %1, off sc1\n\ts_nop 1" ::"v"(p), "v"(v) : "memory"); }
DI float dot2(unsigned a, unsigned b, float c) { return __builtin_amdgcn_fdot2_f32_bf16(__builtin_bit_cast(bf16x2, a), __builtin_bit_cast(bf16x2, b), c, false); }
#define BAR() asm volatile("s_waitcnt lgkmcnt(0)\n\ts_barrier" ::: "memory")
template <int N> DI void waitvm() { asm volatile("s_waitcnt vmcnt(%0)" ::"n"(N) : "memory"); }
DI unsigned pk2(float lo, float hi) { f32x2 v = {lo, hi}; bf16x2 b = __builtin_convertvector(v, bf16x2); return __builtin_bit_cast(unsigned, b); }
DI unsigned pk2v(f32x2 v) { bf16x2 b = __builtin_convertvector(v, bf16x2); return __builtin_bit_cast(unsigned, b); }
DI float bflo(unsigned u) { return __uint_as_float(u << 16); }
DI float bfhi(unsigned u) { return __uint_as_float(u & 0xffff0000u); }
DI f32x2 unpk(unsigned u) { return f32x2{bflo(u), bfhi(u)}; }
DI float ex2(float x) { return __builtin_amdgcn_exp2f(x); }
DI float rcp(float x) { return __builtin_amdgcn_rcpf(x); }
DI f32x2 lo2(f32x4 v) { return f32x2{v[0], v[1]}; }
DI f32x2 hi2(f32x4 v) { return f32x2{v[2], v[3]}; }
DI f32x2 gelu2(f32x2 t) {
  const float c1 = -1.5957691216057308f * 1.4426950408889634f, c2 = c1 * 0.044715f;
  f32x2 u = t * t;
  f32x2 z = t * (u * c2 + c1);
  f32x2 d = f32x2{ex2(z[0]), ex2(z[1])} + 1.0f;
  return t * f32x2{rcp(d[0]), rcp(d[1])};
}
DI f32x2 phi2(f32x2 t) {
  f32x2 a = t + (1.0f + 1e-6f), z = t * 1.4426950408889634f;
  f32x2 e = f32x2{ex2(z[0]), ex2(z[1])} + 1e-6f;
  return f32x2{t[0] > 0.f ? a[0] : e[0], t[1] > 0.f ? a[1] : e[1]};
}
DI float shx(float v, int m) { return __shfl_xor(v, m, 64); }
DI bf16x8 lds128(LAS const char* p) { return *(LAS const bf16x8*)p; }
DI v4i16 ldstr(LAS const char* p) { return __builtin_amdgcn_ds_read_tr16_b64_v4i16((LAS v4i16*)p); }
DI bf16x8 cat(v4i16 a, v4i16 b) { v8i16 r = __builtin_shufflevector(a, b, 0, 1, 2, 3, 4, 5, 6, 7); return __builtin_bit_cast(bf16x8, r); }
DI f32x4 mfma(bf16x8 a, bf16x8 b, f32x4 c) { return __builtin_amdgcn_mfma_f32_16x16x32_bf16(a, b, c, 0, 0, 0); }

template <int MB_, int NB_, int NB0_, int KS_, int D_R, bool XROW_, bool FRAGW_>
struct Cfg {
  static constexpr int MB = MB_, NB = NB_, NB0 = NB0_, KS = KS_, D = D_R;
  static constexpr bool XROW = XROW_, FRAGW = FRAGW_;
  static constexpr int MBW = MB / 2, RB = NB * 32, TOKB = MB * KS * 1024, WB = KS * 32 * RB, STAGE = TOKB + WB;
  static constexpr int PT = TOKB / 4096, PW = WB / 4096, P = PT + PW;
};
typedef Cfg<8, 8, 4, 2, 4, true, false> CfgA1;
typedef Cfg<8, 8, 4, 2, 4, false, false> CfgA;
typedef Cfg<4, 10, 5, 2, 4, true, false> CfgQ;
typedef Cfg<8, 10, 5, 2, 4, false, true> CfgS;
typedef Cfg<4, 5, 3, 4, 4, false, true> CfgC;
constexpr int LDS_AUX = 147456;
constexpr int LDS_TOTAL = LDS_AUX + 4096;
#define RLX __ATOMIC_RELAXED, __HIP_MEMORY_SCOPE_AGENT

template <int RB> DI unsigned wswz(unsigned rho) { return RB == 256 ? 2 * (rho & 7) : (RB == 320 ? 2 * ((rho >> 2) & 1) : 0); }

template <class C, int XT, int XE, class Pre, class Post, class Ext, class Fin>
DI void loader(int w, unsigned lds0, const char* tokbase, int tokstride, const unsigned (&tokv)[C::PT],
               const char* wb, int wstride, const unsigned (&wv)[C::PW], int NS, Pre pre, Post post, Ext ext, Fin fin) {
  static_assert(C::D == 4, "ring depth");
  auto issue = [&](int s) {
    const char* tb = tokbase + (size_t)s * tokstride;
    const char* wp = wb + (size_t)s * wstride;
    unsigned dst = lds0 + (s & 3) * C::STAGE + w * 1024;
#pragma unroll
    for (int i = 0; i < C::PT; i++) dma16(tb, tokv[i], dst + i * 4096);
#pragma unroll
    for (int i = 0; i < C::PW; i++) dma16(wp, wv[i], dst + C::TOKB + i * 4096);
  };
  issue(0); issue(1); pre();
  waitvm<C::P + XT>(); BAR();
  issue(2); issue(3); post(0);
  waitvm<2 * C::P + XT>(); BAR();
  issue(4); post(1);
#pragma unroll 1
  for (int j = 2; j < NS - 3; j++) {
    waitvm<2 * C::P>(); BAR();
    issue(j + 3); post(j);
  }
  waitvm<2 * C::P>(); BAR();
  ext(lds0 + (NS & 3) * C::STAGE); post(NS - 3);
  waitvm<C::P + XE>(); BAR();
  post(NS - 2);
  waitvm<XE>(); BAR();
  post(NS - 1);
  fin();
}
struct Nop { DI void operator()() const {} DI void operator()(int) const {} DI void operator()(unsigned) const {} };

template <class C> DI void make_wv(unsigned (&wv)[C::PW], unsigned w, unsigned lane, unsigned pitch) {
#pragma unroll
  for (int i = 0; i < C::PW; i++) {
    if (C::FRAGW) {
      unsigned pc = i * 4 + w, ks = pc / C::NB, nb = pc % C::NB;
      wv[i] = (ks * pitch + nb) * 1024 + lane * 16;
    } else {
      unsigned sidx = (i * 4 + w) * 64 + lane;
      constexpr unsigned SL = C::RB / 16;
      unsigned rho = sidx / SL, c = sidx % SL;
      unsigned k = C::XROW ? ((rho & ~31u) | (((rho >> 2) & 3) << 3) | (((rho >> 4) & 1) << 2) | (rho & 3)) : rho;
      wv[i] = k * pitch + ((c ^ wswz<C::RB>(rho)) << 4);
    }
  }
}
template <class C> DI void make_tokv(unsigned (&tv)[C::PT], unsigned w, unsigned lane, unsigned kstot) {
#pragma unroll
  for (int i = 0; i < C::PT; i++) {
    unsigned qd = i * 4 + w;
    if (C::XROW) { unsigned row = qd * 8 + (lane >> 3); tv[i] = row * 1280 + (((lane & 7) ^ ((row >> 1) & 7)) << 4); }
    else { unsigned mb = qd / C::KS, ks = qd % C::KS; tv[i] = (mb * kstot + ks) * 1024 + lane * 16; }
  }
}

template <class C, int NBW> struct Frags { bf16x8 t[C::MBW]; v4i16 wl[NBW], wh[NBW]; bf16x8 wf[NBW]; };
template <class C> struct CAddr { int tok[2]; int tokb[2]; int wa[4]; };
template <class C> DI void make_caddr(CAddr<C>& a, int wm, int wn, int lane, int pi = 0, int pj = 0) {
  const int g = lane >> 4, q = (lane >> 2) & 3, p = lane & 3, t = lane & 15;
  if (C::XROW) {
    a.tok[0] = (wm * C::MBW * 16 + t) * 128 + ((g ^ ((t >> 1) & 7)) << 4);
    a.tok[1] = (wm * C::MBW * 16 + t) * 128 + (((4 + g) ^ ((t >> 1) & 7)) << 4);
  } else { a.tok[0] = wm * C::MBW * C::KS * 1024 + lane * 16; a.tok[1] = 0; }
  {
    const int rot = pi * (C::XROW ? 2048 : C::KS * 1024);
    a.tokb[0] = a.tok[0] - rot; a.tokb[1] = a.tok[1] - rot; a.tok[0] += rot; a.tok[1] += rot;
  }
  int r0 = 4 * g + q, base = C::TOKB + r0 * C::RB + (p >> 1) * 16 + (p & 1) * 8;
  if (C::FRAGW) { a.wa[0] = C::TOKB + wn * C::NB0 * 1024 + lane * 16; a.wa[1] = a.wa[2] = a.wa[3] = 0; }
  else if (C::RB == 256) {
#pragma unroll
    for (int j = 0; j < 4; j++) a.wa[j] = base + (((wn * 4 + ((j + pj) & 3)) ^ (r0 & 7)) << 5);
  } else {
    int b = g & 1, n0 = wn * C::NB0;
    a.wa[0] = base + ((n0 ^ b) << 5);
    a.wa[1] = base + (((n0 + 1) ^ b) << 5) - 32;
    a.wa[2] = a.wa[3] = 0;
  }
}
template <class C, int NBW> struct RD { static constexpr int WR = C::FRAGW ? 1 : 2, NR = C::MBW + WR * NBW; };
template <class C, int NBW>
DI void read_one(Frags<C, NBW>& f, LAS const char* slot, const CAddr<C>& a, int ks, int r) {
  constexpr int WR = RD<C, NBW>::WR;
  int ti = -1, wj = -1, h = 0;
  if (r == 0) ti = 0;
  else if (r <= WR) { wj = 0; h = r - 1; }
  else if (r < WR + C::MBW) ti = r - WR;
  else { int q = r - C::MBW; wj = q / WR; h = q % WR; }
  if (ti >= 0) {
    const bool hi = ti >= C::MBW / 2;
    f.t[ti] = C::XROW ? lds128(slot + (hi ? a.tokb[ks & 1] : a.tok[ks & 1]) + ti * 2048)
                      : lds128(slot + (hi ? a.tokb[0] : a.tok[0]) + (ti * C::KS + ks) * 1024);
  } else if (C::FRAGW) {
    f.wf[wj] = lds128(slot + a.wa[0] + (ks * C::NB + wj) * 1024);
  } else {
    LAS const char* pw = C::RB == 256 ? slot + a.wa[wj] : slot + a.wa[wj & 1] + wj * 32;
    if (h == 0) f.wl[wj] = ldstr(pw + ks * 32 * C::RB);
    else f.wh[wj] = ldstr(pw + (ks * 32 + 16) * C::RB);
  }
}
template <class C, int NBW> DI bf16x8 wfrag(const Frags<C, NBW>& f, int j) { return C::FRAGW ? f.wf[j] : cat(f.wl[j], f.wh[j]); }
template <class C, int NBW>
DI void load_all(Frags<C, NBW>& f, LAS const char* slot, const CAddr<C>& a, int ks) {
#pragma unroll
  for (int r = 0; r < RD<C, NBW>::NR; r++) read_one<C, NBW>(f, slot, a, ks, r);
}
template <int NCS_, int CSM_, int NTS_> struct Extra {
  static constexpr int NCS = NCS_, CSM = CSM_, NTS = NTS_;
  f32x4 cs[NCS_ ? NCS_ : 1], ts[NTS_ ? NTS_ : 1], tq[NTS_ ? NTS_ : 1];
};
template <class C, int NBW, bool SWAP, bool BARR, bool RDN, class E>
DI void kstep(f32x4 (&acc)[C::MBW][NBW], E& ex, const Frags<C, NBW>& cur, Frags<C, NBW>& nxt,
              LAS const char* nslot, const CAddr<C>& a, int nks, bf16x8 ones) {
  constexpr int MBW = C::MBW, NM = MBW * NBW, NR = RD<C, NBW>::NR;
  constexpr int F0 = BARR ? MBW : 0, NSL = NM - F0, PER = (NR + NSL - 1) / NSL;
#pragma unroll
  for (int m = 0; m < NM; m++) {
    const int j = m / MBW, i = m % MBW;
    bf16x8 wf = wfrag<C, NBW>(cur, j);
    acc[i][j] = SWAP ? mfma(wf, cur.t[i], acc[i][j]) : mfma(cur.t[i], wf, acc[i][j]);
    if (i == MBW - 1 && j < E::NCS) ex.cs[j] = E::CSM ? mfma(wf, ones, ex.cs[j]) : mfma(ones, wf, ex.cs[j]);
    if (j == 2 && i < E::NTS) ex.ts[i] = mfma(ones, cur.t[i], ex.ts[i]);
    if (j == 3 && i < E::NTS) ex.tq[i] = mfma(cur.t[i], cur.t[i], ex.tq[i]);
    if (BARR && m == F0 - 1) { SB(); BAR(); }
    if (RDN && m >= F0) {
#pragma unroll
      for (int r = (m - F0) * PER; r < (m - F0 + 1) * PER && r < NR; r++) read_one<C, NBW>(nxt, nslot, a, nks, r);
    }
    SB();
  }
}
template <class C, int NBW, bool SWAP, bool LAST, class E>
DI void consume(f32x4 (&acc)[C::MBW][NBW], E& ex, Frags<C, NBW>& fa, LAS const char* lds, const CAddr<C>& a, int s0, int s1, bf16x8 ones) {
  Frags<C, NBW> fb;
  const int e = LAST ? s1 - 1 : s1;
#pragma unroll 1
  for (int s = s0; s < e; s++) {
    LAS const char* slot = lds + (s & 3) * C::STAGE;
    LAS const char* nsl = lds + ((s + 1) & 3) * C::STAGE;
#pragma unroll
    for (int ks = 0; ks < C::KS; ks += 2) {
      kstep<C, NBW, SWAP, false, true>(acc, ex, fa, fb, slot, a, ks + 1, ones);
      if (ks + 2 < C::KS) kstep<C, NBW, SWAP, false, true>(acc, ex, fb, fa, slot, a, ks + 2, ones);
      else kstep<C, NBW, SWAP, true, true>(acc, ex, fb, fa, nsl, a, 0, ones);
    }
  }
  if (LAST) {
    LAS const char* slot = lds + (e & 3) * C::STAGE;
#pragma unroll
    for (int ks = 0; ks < C::KS; ks += 2) {
      kstep<C, NBW, SWAP, false, true>(acc, ex, fa, fb, slot, a, ks + 1, ones);
      if (ks + 2 < C::KS) kstep<C, NBW, SWAP, false, true>(acc, ex, fb, fa, slot, a, ks + 2, ones);
      else kstep<C, NBW, SWAP, false, false>(acc, ex, fb, fa, slot, a, 0, ones);
    }
  }
}
template <int A, int B> DI void zero(f32x4 (&acc)[A][B]) {
#pragma unroll
  for (int i = 0; i < A; i++)
#pragma unroll
    for (int j = 0; j < B; j++) acc[i][j] = f32x4{0.f, 0.f, 0.f, 0.f};
}
template <int A, int M, int B> DI void zero(Extra<A, M, B>& e) {
#pragma unroll
  for (int i = 0; i < (A ? A : 1); i++) e.cs[i] = f32x4{0.f, 0.f, 0.f, 0.f};
#pragma unroll
  for (int i = 0; i < (B ? B : 1); i++) e.ts[i] = e.tq[i] = f32x4{0.f, 0.f, 0.f, 0.f};
}
DI bf16x8 ones_frag() { u32x4 o = {0x3f803f80u, 0x3f803f80u, 0x3f803f80u, 0x3f803f80u}; return __builtin_bit_cast(bf16x8, o); }
#define CBAR() asm volatile("s_waitcnt lgkmcnt(0)\n\ts_barrier" ::: "memory")

extern __shared__ __attribute__((aligned(16))) char smem[];
#define AUXF ((LAS float*)(lds + LDS_AUX))
DI f32x4 ldsf4(LAS const float* p) { return *(LAS const f32x4*)p; }
DI float sel4(f32x4 v, int k) { float a = (k & 1) ? v[1] : v[0], b = (k & 1) ? v[3] : v[2]; return (k & 2) ? b : a; }

extern "C" __global__ __launch_bounds__(512) void k_phase1(const char* __restrict__ x, const char* __restrict__ wqkv, const char* __restrict__ w1, char* __restrict__ ws,
                                              const char* __restrict__ wo, const char* __restrict__ w2, const char* __restrict__ w3, const char* __restrict__ w4) {
  typedef CfgA1 C;
  const int b = blockIdx.x, xcd = b & 7, idx = b >> 3;
  const int rt = (xcd >> 1) * 4 + (idx & 3), ct = (xcd & 1) * 8 + (idx >> 2);
  const int wid = __builtin_amdgcn_readfirstlane(threadIdx.x >> 6), lane = threadIdx.x & 63;
  LAS char* lds = (LAS char*)smem;
  STAMP(0, 0);
  if (ct >= 15) return;
  if (wid >= 4) {
    const int w = wid - 4;
    unsigned tv[C::PT], wv[C::PW];
    const char* wb = ct < 10 ? wqkv + (640 + ct * 128) * 2 : w1 + (ct - 10) * 256;
    const int pitch = ct < 10 ? 3840 : 1280;
    make_tokv<C>(tv, w, lane, 0); make_wv<C>(wv, w, lane, pitch);
    float s[4] = {0.f, 0.f, 0.f, 0.f}, q[4] = {0.f, 0.f, 0.f, 0.f};
    LAS const char* my = lds + w * 1024 + lane * 16;
    auto post = [&](int j) {
      LAS const char* p = my + (j & 3) * C::STAGE;
#pragma unroll
      for (int i = 0; i < 4; i++) {
        u32x4 v = *(LAS const u32x4*)(p + i * 4096);
#pragma unroll
        for (int e = 0; e < 4; e++) { s[i] = dot2(v[e], 0x3f803f80u, s[i]); q[i] = dot2(v[e], v[e], q[i]); }
      }
    };
    auto fin = [&]() {
#pragma unroll
      for (int i = 0; i < 4; i++) {
#pragma unroll
        for (int m = 1; m < 8; m <<= 1) { s[i] += shx(s[i], m); q[i] += shx(q[i], m); }
      }
      const int i = lane & 3;
      const float sv = sel4(f32x4{s[0], s[1], s[2], s[3]}, i), qv = sel4(f32x4{q[0], q[1], q[2], q[3]}, i);
      const float m = sv * (1.0f / D_), var = fmaxf(qv * (1.0f / D_) - m * m, 0.f);
      const float rs = __builtin_amdgcn_rsqf(var + EPS);
      float r = rs;
      if (ct >= 10) r = rs * __builtin_amdgcn_rsqf(var * rs * rs + EPS);
      const int row = (i * 4 + w) * 8 + (lane >> 3);
      if ((lane & 7) < 4) {
        AUXF[row] = r; AUXF[128 + row] = r * m;
        if (ct == 0) ((f32x2*)(ws + OFF_ST))[rt * 128 + row] = f32x2{m, rs};
      }
      BAR();
    };
    const unsigned junk = (unsigned)(size_t)lds + LDS_AUX + 3584;
    auto pre = [&]() {
      const unsigned gid = min(b * 4 + w, 719);
      const char* p1 = gid < 320 ? x + gid * 8192 : (gid < 620 ? wqkv + (gid - 320) * 8192 : w1 + (gid - 620) * 8192);
      dma4(p1, lane * 128, junk);
      touch_code(idx * 4 + w, lane, junk);
    };
    loader<C, 1 + XC, 0>(w, (unsigned)(size_t)lds, x + (size_t)rt * 128 * 1280, 128, tv, wb, 64 * pitch, wv, 10, pre, post, Nop(), fin);
    waitvm<0>();
    STAMP(0, 1);
    return;
  }
  const int wm = wid >> 1, wn = wid & 1, g = lane >> 4, tk = lane & 15;
  CAddr<C> a; make_caddr<C>(a, wm, wn, lane, 0, 2 * wm);
  f32x4 acc[4][4]; zero(acc);
  Extra<2, 0, 0> ex; zero(ex);
  const bf16x8 ones = ones_frag();
  Frags<C, 4> fa;
  CBAR(); STAMP(0, 1); CSTAMP(0, 6); load_all<C, 4>(fa, lds, a, 0);
  if (ct < 10) consume<C, 4, false, true>(acc, ex, fa, lds, a, 0, 10, ones);
  else consume<C, 4, true, true>(acc, ex, fa, lds, a, 0, 10, ones);
  STAMP(0, 2); CSTAMP(0, 7);
  {
    const unsigned gid = b * 4 + wid;
    const char* p2 = gid < 100 ? wo + gid * 8192 : (gid < 200 ? w2 + (gid - 100) * 8192 : (gid < 440 ? w3 + (gid - 200) * 8192 : w4 + (gid - 440) * 8192));
    if (gid < 680) dma4(p2, lane * 128, (unsigned)(size_t)lds + LDS_AUX + 3584);
  }
  if (g == 0) {
#pragma unroll
    for (int i = 0; i < 2; i++) AUXF[256 + wn * 64 + (2 * wm + i) * 16 + tk] = ex.cs[i][0];
  }
  CBAR();
  STAMP(0, 3);
  if (ct < 10) {
    char* dst = ws + (ct < 5 ? OFF_PK : OFF_V) + lane * 16;
    const int cb0 = (ct < 5 ? ct : ct - 5) * 8 + wn * 4;
    float cs[4]; f32x2 zs[4];
#pragma unroll
    for (int j = 0; j < 4; j++) { cs[j] = AUXF[256 + wn * 64 + ((j + 2 * wm) & 3) * 16 + tk]; zs[j] = f32x2{0.f, 0.f}; }
#pragma unroll
    for (int ip = 0; ip < 2; ip++) {
      const int tb = wm * 64 + ip * 32 + 4 * g;
      f32x4 r0 = ldsf4(AUXF + tb), r1 = ldsf4(AUXF + tb + 16), m0 = ldsf4(AUXF + 128 + tb), m1 = ldsf4(AUXF + 128 + tb + 16);
      char* d2 = dst + (size_t)((rt * 4 + wm * 2 + ip) * 40 + cb0) * 1024;
#pragma unroll
      for (int j = 0; j < 4; j++) {
        f32x4 t0 = r0 * acc[2 * ip][j] - m0 * cs[j], t1 = r1 * acc[2 * ip + 1][j] - m1 * cs[j];
        u32x4 o;
        if (ct < 5) {
          o = u32x4{pk2v(phi2(lo2(t0))), pk2v(phi2(hi2(t0))), pk2v(phi2(lo2(t1))), pk2v(phi2(hi2(t1)))};
          zs[j] += (unpk(o[0]) + unpk(o[1])) + (unpk(o[2]) + unpk(o[3]));
        } else {
          o = u32x4{pk2v(lo2(t0)), pk2v(hi2(t0)), pk2v(lo2(t1)), pk2v(hi2(t1))};
        }
        st16(d2 + ((j + 2 * wm) & 3) * 1024, o);
      }
    }
    if (ct < 5) {
      float z[4];
#pragma unroll
      for (int j = 0; j < 4; j++) { z[j] = zs[j][0] + zs[j][1]; z[j] += shx(z[j], 16); z[j] += shx(z[j], 32); }
      if (wm == 1 && lane < 16) {
#pragma unroll
        for (int j = 0; j < 4; j++) AUXF[704 + wn * 64 + ((j + 2) & 3) * 16 + lane] = z[j];
      }
      CBAR();
      if (wm == 0 && lane < 16) {
        float* zp = (float*)(ws + OFF_ZP) + rt * 640 + ct * 128 + wn * 64;
#pragma unroll
        for (int j = 0; j < 4; j++) zp[j * 16 + lane] = z[j] + AUXF[704 + wn * 64 + j * 16 + lane];
      }
    }
  } else {
    const int c0 = (ct - 10) * 128 + wn * 64;
#pragma unroll
    for (int i = 0; i < 4; i++) {
      const int row = wm * 64 + i * 16 + tk;
      const float rr = AUXF[row], rm = AUXF[128 + row];
#pragma unroll
      for (int jp = 0; jp < 2; jp++) {
        const int jt = jp ^ wm;
        u32x4 o;
#pragma unroll
        for (int h = 0; h < 2; h++) {
          f32x4 cs = ldsf4(AUXF + 256 + wn * 64 + (2 * jt + h) * 16 + 4 * g);
          f32x4 t = rr * acc[i][2 * jp + h] - rm * cs;
          o[2 * h] = pk2v(gelu2(lo2(t))); o[2 * h + 1] = pk2v(gelu2(hi2(t)));
        }
        const int col = c0 + jt * 32, grp = col / 160;
        const int dl = grp == 0 ? 1 : (grp == 1 ? -1 : (grp == 2 ? 2 : -2));
        int td = rt * 128 + row + dl;
        const bool ok = td >= 0 && td < S_;
        td &= S_ - 1;
        if (!ok) o = u32x4{0, 0, 0, 0};
        st16(ws + OFF_H + ((size_t)((td >> 4) * 20 + (col >> 5)) * 64 + g * 16 + (td & 15)) * 16, o);
      }
    }
  }
  waitvm<0>();
  STAMPEND(0);
}

extern "C" __global__ __launch_bounds__(512) void k_phase2(const char* __restrict__ x, const char* __restrict__ wqkv, const char* __restrict__ wo, const char* __restrict__ w2,
                                              const char* __restrict__ w4, char* __restrict__ ws) {
  const int b = blockIdx.x;
  const int wid = __builtin_amdgcn_readfirstlane(threadIdx.x >> 6), lane = threadIdx.x & 63;
  LAS char* lds = (LAS char*)smem;
  const int g = lane >> 4, tk = lane & 15;
  STAMP(1, 0);
  if (b < 128) {
    typedef CfgQ C;
    const int rt = b >> 2, ct = b & 3;
    if (wid >= 4) {
      const int w = wid - 4;
      unsigned tv[C::PT], wv[C::PW];
      make_tokv<C>(tv, w, lane, 0); make_wv<C>(wv, w, lane, 3840);
      loader<C, XC, 0>(w, (unsigned)(size_t)lds, x + (size_t)rt * 64 * 1280, 128, tv, wqkv + ct * 320, 64 * 3840, wv, 10, [&]() { touch_code((b >> 3) * 4 + w, lane, (unsigned)(size_t)lds + LDS_AUX + 3584); }, Nop(), Nop(), Nop());
      STAMP(1, 1);
      return;
    }
    const int wm = wid >> 1, wn = wid & 1;
    float zp[16];
    const int t = threadIdx.x;
    if (t < 160) {
      const float* p = (const float*)(ws + OFF_ZP) + ct * 160 + t;
#pragma unroll
      for (int r = 0; r < 16; r++) zp[r] = p[r * 640];
    }
    f32x2 st[2];
#pragma unroll
    for (int i = 0; i < 2; i++) st[i] = ((const f32x2*)(ws + OFF_ST))[rt * 64 + wm * 32 + i * 16 + tk];
    CAddr<C> a; make_caddr<C>(a, wm, wn, lane);
    f32x4 acc[2][5]; zero(acc);
    Extra<5, 1, 0> ex; zero(ex);
    const bf16x8 ones = ones_frag();
    Frags<C, 5> fa;
    CBAR(); STAMP(1, 1); CSTAMP(1, 6); load_all<C, 5>(fa, lds, a, 0);
    if (t < 160) {
      float z = 0.f;
#pragma unroll
      for (int r = 0; r < 16; r += 4) z += (zp[r] + zp[r + 1]) + (zp[r + 2] + zp[r + 3]);
      AUXF[512 + t] = z;
    }
    consume<C, 5, true, true>(acc, ex, fa, lds, a, 0, 10, ones);
    STAMP(1, 2); CSTAMP(1, 7);
#pragma unroll
    for (int i = 0; i < 2; i++) {
      const float rs = st[i][1], rm = rs * st[i][0];
      f32x4 pq[5]; float part[5];
#pragma unroll
      for (int j = 0; j < 5; j++) {
        f32x4 z = ldsf4(AUXF + 512 + wn * 80 + j * 16 + 4 * g);
        f32x4 tt = rs * acc[i][j] - rm * ex.cs[j];
        f32x2 p0 = phi2(lo2(tt)), p1 = phi2(hi2(tt));
        pq[j] = f32x4{p0[0], p0[1], p1[0], p1[1]};
        f32x2 s = p0 * lo2(z) + p1 * hi2(z);
        part[j] = s[0] + s[1];
      }
      float dh[4];
      dh[0] = part[0] + (g == 0 ? part[1] : 0.f);
      dh[1] = (g >= 1 ? part[1] : 0.f) + (g <= 1 ? part[2] : 0.f);
      dh[2] = (g >= 2 ? part[2] : 0.f) + (g <= 2 ? part[3] : 0.f);
      dh[3] = (g == 3 ? part[3] : 0.f) + part[4];
#pragma unroll
      for (int h = 0; h < 4; h++) { dh[h] += shx(dh[h], 16); dh[h] += shx(dh[h], 32); dh[h] = rcp(dh[h] + 1e-6f); }
      const float sc[5] = {dh[0], g == 0 ? dh[0] : dh[1], g <= 1 ? dh[1] : dh[2], g <= 2 ? dh[2] : dh[3], dh[3]};
#pragma unroll
      for (int j = 0; j < 5; j++) pq[j] *= sc[j];
      char* dst = ws + OFF_PQ + ((size_t)(rt * 4 + wm * 2 + i) * 20 + ct * 5) * 1024 + lane * 16;
      st16(dst + (3 * wn) * 1024, u32x4{pk2v(lo2(pq[0])), pk2v(hi2(pq[0])), pk2v(lo2(pq[1])), pk2v(hi2(pq[1]))});
      st16(dst + (3 * wn + 1) * 1024, u32x4{pk2v(lo2(pq[2])), pk2v(hi2(pq[2])), pk2v(lo2(pq[3])), pk2v(hi2(pq[3]))});
      *(u32x2*)(dst + 2 * 1024 + wn * 8) = u32x2{pk2v(lo2(pq[4])), pk2v(hi2(pq[4]))};
    }
    STAMPEND(1);
  } else if (b < 208) {
    typedef CfgA C;
    const int bb = b - 128, rt = (bb & 7) * 2 + (bb >> 3) / 5, ct = (bb >> 3) % 5;
    if (wid >= 4) {
      const int w = wid - 4;
      unsigned tv[C::PT], wv[C::PW];
      make_tokv<C>(tv, w, lane, 20); make_wv<C>(wv, w, lane, 1280);
      const char* xb = x + (size_t)rt * 128 * 1280 + ct * 256;
      const unsigned xr = w * 4 + g, xo = xr * 1280 + ((tk ^ xr) << 4);
      auto ext = [&](unsigned dst) {
#pragma unroll
        for (int i = 0; i < 8; i++) dma16(xb, xo + i * 16 * 1280, dst + (i * 4 + w) * 1024);
      };
      auto fin = [&]() { waitvm<0>(); BAR(); };
      loader<C, 0, 8>(w, (unsigned)(size_t)lds, ws + OFF_H + (size_t)rt * 8 * 20 * 1024, 2048, tv, w2 + ct * 256, 64 * 1280, wv, 10, Nop(), Nop(), ext, fin);
      STAMP(1, 1);
      return;
    }
    const int wm = wid >> 1, wn = wid & 1;
    f32x2 st[4];
#pragma unroll
    for (int i = 0; i < 4; i++) st[i] = ((const f32x2*)(ws + OFF_ST))[rt * 128 + wm * 64 + i * 16 + tk];
    CAddr<C> a; make_caddr<C>(a, wm, wn, lane);
    f32x4 acc[4][4]; zero(acc);
    Extra<0, 0, 0> ex;
    const bf16x8 ones = ones_frag();
    Frags<C, 4> fa;
    CBAR(); STAMP(1, 1); CSTAMP(1, 6); load_all<C, 4>(fa, lds, a, 0);
    consume<C, 4, true, true>(acc, ex, fa, lds, a, 0, 10, ones);
    STAMP(1, 2); CSTAMP(1, 7);
    CBAR();
    LAS const char* xs = lds + 2 * C::STAGE + (wm * 64 + tk) * 256 + (g & 1) * 8;
#pragma unroll
    for (int i = 0; i < 4; i++) {
      const float rs = st[i][1], rm = st[i][0] * rs;
      f32x2 s1 = {0.f, 0.f}, s2 = {0.f, 0.f};
      char* dst = ws + OFF_X1 + ((size_t)(rt * 8 + wm * 4 + i) * 20 + ct * 4 + wn * 2) * 1024 + lane * 16;
#pragma unroll
      for (int jp = 0; jp < 2; jp++) {
        u32x4 o;
#pragma unroll
        for (int h = 0; h < 2; h++) {
          u32x2 xx = *(LAS const u32x2*)(xs + i * 16 * 256 + (((wn * 8 + (2 * jp + h) * 2 + (g >> 1)) ^ tk) << 4));
          f32x4 c = acc[i][2 * jp + h] - rm;
          o[2 * h] = pk2v(unpk(xx[0]) * rs + lo2(c));
          o[2 * h + 1] = pk2v(unpk(xx[1]) * rs + hi2(c));
        }
#pragma unroll
        for (int e = 0; e < 4; e++) { f32x2 v = unpk(o[e]); s1 += v; s2 += v * v; }
        st16(dst + jp * 1024, o);
      }
      float a1 = s1[0] + s1[1], a2 = s2[0] + s2[1];
      a1 += shx(a1, 16); a2 += shx(a2, 16); a1 += shx(a1, 32); a2 += shx(a2, 32);
      if (g == 0) ((f32x2*)(ws + OFF_XS))[(ct * 2 + wn) * 2048 + rt * 128 + wm * 64 + i * 16 + tk] = f32x2{a1, a2};
    }
    STAMPEND(1);
  } else if (b < 240) {
    const int h = b - 208, hh = h & 3, cb0 = 5 * (h >> 2) + hh;
    LAS char* wt = lds + 40960;
    {
      const char* wb = wo + (size_t)cb0 * 16 * 1280;
#pragma unroll
      for (int i = 0; i < 5; i++) {
        const unsigned pc = i * 8 + wid, s = pc * 64 + lane, qt = s / 640, r = (s % 640) / 20, c = s % 20;
        dma16(wb, r * 1280 + qt * 320 + ((c ^ (2 * ((r >> 2) & 1))) << 4), (unsigned)(size_t)wt + pc * 1024);
      }
    }
    bf16x8 pk[8][2], vv[8][2];
#pragma unroll
    for (int u = 0; u < 8; u++)
#pragma unroll
      for (int j = 0; j < 2; j++) {
        const size_t o = ((size_t)(wid * 8 + u) * 40 + cb0 + j) * 1024 + lane * 16;
        pk[u][j] = *(const bf16x8*)(ws + OFF_PK + o); vv[u][j] = *(const bf16x8*)(ws + OFF_V + o);
      }
    f32x4 acc[4];
#pragma unroll
    for (int i = 0; i < 4; i++) acc[i] = f32x4{0.f, 0.f, 0.f, 0.f};
#pragma unroll
    for (int u = 0; u < 8; u++)
#pragma unroll
      for (int i = 0; i < 4; i++) acc[i] = mfma(vv[u][i & 1], pk[u][i >> 1], acc[i]);
    STAMP(1, 2);
#pragma unroll
    for (int i = 0; i < 4; i++) *(LAS f32x4*)(lds + (wid * 4 + i) * 1024 + lane * 16) = acc[i];
    waitvm<0>();
    BAR();
    if (wid < 4) {
      f32x4 s = *(LAS const f32x4*)(lds + wid * 1024 + lane * 16);
#pragma unroll
      for (int w = 1; w < 8; w++) s += *(LAS const f32x4*)(lds + (w * 4 + wid) * 1024 + lane * 16);
      const unsigned ee = 4 * (wid & 1) + g - hh;
      u32x2 o = u32x2{pk2v(lo2(s)), pk2v(hi2(s))};
      if (ee >= 5u) o = u32x2{0u, 0u};
      *(LAS u32x2*)(lds + 32768 + wid * 512 + lane * 8) = o;
    }
    BAR();
    STAMP(1, 3);
    {
      const int q = (lane >> 2) & 3, p = lane & 3;
      bf16x8 af[2]; char* dst[2]; bool ok[2];
#pragma unroll
      for (int db = 0; db < 2; db++) {
        const u32x2 h0 = *(LAS const u32x2*)(lds + 32768 + (db * 2) * 512 + lane * 8), h1 = *(LAS const u32x2*)(lds + 32768 + (db * 2 + 1) * 512 + lane * 8);
        af[db] = __builtin_bit_cast(bf16x8, u32x4{h0[0], h0[1], h1[0], h1[1]});
        const int beta = cb0 + db, GG = beta / 10, j = beta % 10, wn = j / 5, jj = j % 5;
        const int kap = jj == 4 ? 5 * GG + 2 : 5 * GG + 3 * wn + (jj >> 1), tt = jj == 4 ? wn : (jj & 1);
        dst[db] = ws + OFF_ME + (size_t)kap * 40 * 1024 + lane * 16 + tt * 8;
        ok[db] = (unsigned)(4 * db + g - hh) < 5u;
      }
#pragma unroll
      for (int i = 0; i < 5; i++) {
        const int nb = wid * 5 + i, qt = nb / 10, nl = nb % 10;
        LAS const char* src = wt + qt * 10240 + (4 * g + q) * 320 + ((nl ^ (g & 1)) << 5) + p * 8;
        const bf16x8 wf = cat(ldstr(src), ldstr(src + 16 * 320));
#pragma unroll
        for (int db = 0; db < 2; db++) {
          const f32x4 c = mfma(af[db], wf, f32x4{0.f, 0.f, 0.f, 0.f});
          if (ok[db]) *(u32x2*)(dst[db] + nb * 1024) = u32x2{pk2v(lo2(c)), pk2v(hi2(c))};
        }
      }
    }
    STAMPEND(1);
  } else {
    LAS char* base = lds + wid * 16384;
    const unsigned lb = (unsigned)(size_t)base;
    const int q = (lane >> 2) & 3, p = lane & 3, r0 = 4 * g + q;
    const int u = (b - 240) * 8 + wid;
    if (u < 120) {
      const int kb = u / 5, cb = u % 5;
      const char* src = w4 + kb * 64 * 1280 + cb * 256;
      char* dst = ws + OFF_W4 + (kb * 80 + cb * 8) * 1024 + lane * 16;
#pragma unroll
      for (int i = 0; i < 16; i++) {
        const unsigned rho = i * 4 + g;
        dma16(src, rho * 1280 + (((lane & 15) ^ wswz<256>(rho)) << 4), lb + i * 1024);
      }
      waitvm<0>();
      LAS const char* pw = base + r0 * 256 + p * 8;
#pragma unroll
      for (int nb = 0; nb < 8; nb++)
#pragma unroll
        for (int ks = 0; ks < 2; ks++) {
          LAS const char* pp = pw + ((nb ^ (r0 & 7)) << 5) + ks * 32 * 256;
          st16(dst + (ks * 40 + nb) * 1024, __builtin_bit_cast(u32x4, cat(ldstr(pp), ldstr(pp + 16 * 256))));
        }
    }
    STAMPEND(1);
  }
}

extern "C" __global__ __launch_bounds__(512) void k_phase3(const char* __restrict__ w3, char* __restrict__ ws) {
  const int b = blockIdx.x, xcd = b & 7;
  const int wid = __builtin_amdgcn_readfirstlane(threadIdx.x >> 6), lane = threadIdx.x & 63;
  LAS char* lds = (LAS char*)smem;
  const int wm = (wid >> 1) & 1, wn = wid & 1, g = lane >> 4, tk = lane & 15;
  STAMP(2, 0);
  if (b < 192) {
    typedef CfgA C;
    const int idx = b >> 3;
    const int rt = (xcd >> 1) * 4 + (idx & 3), ct = (xcd & 1) * 6 + (idx >> 2);
    if (wid >= 4) {
      const int w = wid - 4;
      unsigned tv[C::PT], wv[C::PW];
      make_tokv<C>(tv, w, lane, 20); make_wv<C>(wv, w, lane, 3072);
      const unsigned junk = (unsigned)(size_t)lds + LDS_AUX + 3584;
      auto pre = [&]() {
        if (L2T) {
          const unsigned t = w * 64 + lane, la = min((idx >> 2) * 171 + min(t, 170u), 1023u), lb = (idx & 3) * 256 + t;
          dma4(ws + OFF_X1 + (size_t)rt * 8 * 20 * 1024, (la >> 7) * 20480 + 4096 + (la & 127) * 128, junk);
          dma4(w3 + ct * 256, (128 + (lb >> 1)) * 3072 + (lb & 1) * 128, junk);
        }
        touch_code(idx * 4 + w, lane, junk);
      };
      loader<C, XC + 2 * L2T, 0>(w, (unsigned)(size_t)lds, ws + OFF_X1 + (size_t)rt * 8 * 20 * 1024, 2048, tv, w3 + ct * 256, 64 * 3072, wv, 10, pre, Nop(), Nop(), Nop());
      STAMP(2, 1);
      return;
    }
    f32x2 xs[10];
    if (wid < 2) {
      const f32x2* p = (const f32x2*)(ws + OFF_XS) + rt * 128 + threadIdx.x;
#pragma unroll
      for (int i = 0; i < 10; i++) xs[i] = p[i * 2048];
    }
    CAddr<C> a; make_caddr<C>(a, wm, wn, lane);
    f32x4 acc[4][4]; zero(acc);
    Extra<4, 1, 0> ex; zero(ex);
    const bf16x8 ones = ones_frag();
    Frags<C, 4> fa;
    CBAR(); STAMP(2, 1); CSTAMP(2, 6); load_all<C, 4>(fa, lds, a, 0);
    if (wid < 2) {
      f32x2 s = ((xs[0] + xs[1]) + (xs[2] + xs[3])) + ((xs[4] + xs[5]) + (xs[6] + xs[7])) + (xs[8] + xs[9]);
      float m = s[0] * (1.0f / D_), var = fmaxf(s[1] * (1.0f / D_) - m * m, 0.f);
      float rs = __builtin_amdgcn_rsqf(var + EPS);
      AUXF[threadIdx.x] = rs; AUXF[128 + threadIdx.x] = rs * m;
    }
    consume<C, 4, true, true>(acc, ex, fa, lds, a, 0, 10, ones);
    STAMP(2, 2); CSTAMP(2, 7);
#pragma unroll
    for (int i = 0; i < 4; i++) {
      const int row = wm * 64 + i * 16 + tk;
      const float rs = AUXF[row], rm = AUXF[128 + row];
      char* dst = ws + OFF_H3 + ((size_t)(rt * 8 + wm * 4 + i) * 48 + ct * 4 + wn * 2) * 1024 + lane * 16;
#pragma unroll
      for (int jp = 0; jp < 2; jp++) {
        u32x4 o;
#pragma unroll
        for (int h = 0; h < 2; h++) {
          f32x4 t = rs * acc[i][2 * jp + h] - rm * ex.cs[2 * jp + h];
          o[2 * h] = pk2v(gelu2(lo2(t))); o[2 * h + 1] = pk2v(gelu2(hi2(t)));
        }
        st16(dst + jp * 1024, o);
      }
    }
    STAMPEND(2);
  } else {
    typedef CfgS C;
    const int idx = (b - 192) >> 3;
    const int rt = (xcd >> 1) * 4 + (idx & 3), ct = (xcd & 1) * 2 + (idx >> 2);
    if (wid >= 4) {
      const int w = wid - 4;
      unsigned tv[C::PT], wv[C::PW];
      make_tokv<C>(tv, w, lane, 20); make_wv<C>(wv, w, lane, 40);
      loader<C, 0, 0>(w, (unsigned)(size_t)lds, ws + OFF_PQ + (size_t)rt * 8 * 20 * 1024, 2048, tv, ws + OFF_ME + ct * 10 * 1024, 80 * 1024, wv, 10, Nop(), Nop(), Nop(), Nop());
      STAMP(2, 1);
      return;
    }
    CAddr<C> a; make_caddr<C>(a, wm, wn, lane);
    f32x4 acc[4][5]; zero(acc);
    Extra<0, 0, 0> ex;
    const bf16x8 ones = ones_frag();
    Frags<C, 5> fa;
    CBAR(); STAMP(2, 1); CSTAMP(2, 6); load_all<C, 5>(fa, lds, a, 0);
    consume<C, 5, true, true>(acc, ex, fa, lds, a, 0, 10, ones);
    STAMP(2, 2); CSTAMP(2, 7);
#pragma unroll
    for (int i = 0; i < 4; i++) {
      char* dst = ws + OFF_SA + ((size_t)(rt * 8 + wm * 4 + i) * 20 + ct * 5) * 1024 + lane * 16;
      u32x2 o[5];
#pragma unroll
      for (int j = 0; j < 5; j++) o[j] = u32x2{pk2v(lo2(acc[i][j])), pk2v(hi2(acc[i][j]))};
      if (wn == 0) {
        st16(dst, u32x4{o[0][0], o[0][1], o[1][0], o[1][1]});
        st16(dst + 1024, u32x4{o[2][0], o[2][1], o[3][0], o[3][1]});
        *(u32x2*)(dst + 2048) = o[4];
      } else {
        *(u32x2*)(dst + 2048 + 8) = o[0];
        st16(dst + 3072, u32x4{o[1][0], o[1][1], o[2][0], o[2][1]});
        st16(dst + 4096, u32x4{o[3][0], o[3][1], o[4][0], o[4][1]});
      }
    }
    STAMPEND(2);
  }
}

template <int NBW>
DI void p4_consumer(char* __restrict__ out, char* __restrict__ ws, LAS char* lds, int rt, int ct, int wm, int wn, int lane) {
  typedef CfgC C;
  const int g = lane >> 4, tk = lane & 15;
  u32x2 sav[2][NBW], x1v[2][NBW];
  CAddr<C> a; make_caddr<C>(a, wm, wn, lane);
  f32x4 h2[2][NBW]; zero(h2);
  Extra<0, 0, 0> ex;
  const bf16x8 ones = ones_frag();
  Frags<C, NBW> fa;
  CBAR(); STAMP(3, 1); CSTAMP(3, 6); load_all<C, NBW>(fa, lds, a, 0);
  consume<C, NBW, true, false>(h2, ex, fa, lds, a, 0, 9, ones);
#pragma unroll
  for (int i = 0; i < 2; i++)
#pragma unroll
    for (int j = 0; j < NBW; j++) {
      const int blk = ct * 5 + wn * 3 + j;
      const size_t o = ((size_t)(rt * 4 + wm * 2 + i) * 20 + (blk >> 1)) * 1024 + lane * 16 + (blk & 1) * 8;
      x1v[i][j] = *(const u32x2*)(ws + OFF_X1 + o);
      sav[i][j] = *(const u32x2*)(ws + OFF_SA + o);
    }
  consume<C, NBW, true, true>(h2, ex, fa, lds, a, 9, 12, ones);
  STAMP(3, 2); CSTAMP(3, 7);
  CBAR();
  LAS char* xs = lds + (wm * 32 + tk) * 160 + wn * 96 + g * 8;
#pragma unroll
  for (int i = 0; i < 2; i++)
#pragma unroll
    for (int j = 0; j < NBW; j++) {
      LAS u32x2* px = (LAS u32x2*)(xs + i * 16 * 160 + j * 32);
      const u32x2 xv = *px;
      u32x2 o;
#pragma unroll
      for (int e = 0; e < 2; e++) {
        f32x2 bb = unpk(x1v[i][j][e]) + (e ? hi2(h2[i][j]) : lo2(h2[i][j]));
        f32x2 z = bb * -1.4426950408889634f;
        f32x2 d = f32x2{ex2(z[0]), ex2(z[1])} + 1.0f;
        f32x2 sl = bb * f32x2{rcp(d[0]), rcp(d[1])};
        o[e] = pk2v(unpk(xv[e]) + unpk(sav[i][j][e]) * sl);
      }
      *px = o;
    }
  CBAR();
  const int t = (wm * 2 + wn) * 64 + lane;
#pragma unroll
  for (int i = 0; i < 3; i++) {
    const int ch = t + 256 * i;
    if (ch < 640) {
      const int row = ch / 10, c = ch % 10;
      *(u32x4*)(out + (size_t)(rt * 64 + row) * 1280 + ct * 160 + c * 16) = *(LAS const u32x4*)(lds + ch * 16);
    }
  }
  STAMPEND(3);
}
extern "C" __global__ __launch_bounds__(512) void k_phase4(const char* __restrict__ x, char* __restrict__ ws, char* __restrict__ out) {
  typedef CfgC C;
  const int b = blockIdx.x, xcd = b & 7, idx = b >> 3;
  const int rt = (xcd >> 1) * 8 + (idx & 7), ct = (xcd & 1) * 4 + (idx >> 3);
  const int wid = __builtin_amdgcn_readfirstlane(threadIdx.x >> 6), lane = threadIdx.x & 63;
  LAS char* lds = (LAS char*)smem;
  STAMP(3, 0);
  if (wid >= 4) {
    const int w = wid - 4;
    unsigned tv[C::PT], wv[C::PW];
    make_tokv<C>(tv, w, lane, 48); make_wv<C>(wv, w, lane, 40);
    const char* xb = x + (size_t)rt * 64 * 1280 + ct * 160;
    auto ext = [&](unsigned dst) {
#pragma unroll
      for (int i = 0; i < 3; i++) {
        const unsigned ch = min((i * 4 + w) * 64 + lane, 639);
        dma16(xb, (ch / 10) * 1280 + (ch % 10) * 16, dst + (i * 4 + w) * 1024);
      }
    };
    auto fin = [&]() { waitvm<0>(); BAR(); };
    const unsigned junk = (unsigned)(size_t)lds + LDS_AUX + 3584;
    auto pre = [&]() {
      if (L2T) {
        const unsigned t = w * 64 + lane;
#pragma unroll
        for (int i = 0; i < 2; i++) {
          const unsigned la = (idx >> 3) * 320 + min(t + i * 256, 319u);
          dma4(ws + OFF_H3 + (size_t)rt * 4 * 48 * 1024, (la / 320) * 49152 + 8192 + (la % 320) * 128, junk);
        }
        const unsigned lb = (idx & 7) * 200 + min(t, 199u);
        dma4(ws + OFF_W4 + ct * 5 * 1024, (8 + lb / 40) * 40960 + (lb % 40) * 128, junk);
      }
      touch_code(idx * 4 + w, lane, junk);
    };
    loader<C, XC + 3 * L2T, 3>(w, (unsigned)(size_t)lds, ws + OFF_H3 + (size_t)rt * 4 * 48 * 1024, 4096, tv, ws + OFF_W4 + ct * 5 * 1024, 4 * 40 * 1024, wv, 12, pre, Nop(), ext, fin);
    STAMP(3, 1);
    return;
  }
  const int wm = wid >> 1, wn = wid & 1;
  if (wn == 0) p4_consumer<3>(out, ws, lds, rt, ct, wm, wn, lane);
  else p4_consumer<2>(out, ws, lds, rt, ct, wm, wn, lane);
}

extern "C" void kernel_launch(void* const* d_in, const int* in_sizes, int n_in, void* d_out, int out_size, void* d_ws, size_t ws_size, hipStream_t stream) {
  const char* x = (const char*)d_in[0]; const char* wqkv = (const char*)d_in[1]; const char* wo = (const char*)d_in[2];
  const char* w1 = (const char*)d_in[4]; const char* w2 = (const char*)d_in[5]; const char* w3 = (const char*)d_in[6]; const char* w4 = (const char*)d_in[7];
#define WSOFF 2
  char* ws = (char*)d_ws;
  if (WSOFF == 1) ws += (ws_size - (32u << 20)) & ~(size_t)0x1fffff;
  if (WSOFF == 2) ws += (ws_size / 2) & ~(size_t)0x1fffff;
  (void)hipFuncSetAttribute((const void*)k_phase1, hipFuncAttributeMaxDynamicSharedMemorySize, LDS_TOTAL);
  (void)hipFuncSetAttribute((const void*)k_phase2, hipFuncAttributeMaxDynamicSharedMemorySize, LDS_TOTAL);
  (void)hipFuncSetAttribute((const void*)k_phase3, hipFuncAttributeMaxDynamicSharedMemorySize, LDS_TOTAL);
  (void)hipFuncSetAttribute((const void*)k_phase4, hipFuncAttributeMaxDynamicSharedMemorySize, LDS_TOTAL);
  k_phase1<<<256, 512, LDS_TOTAL, stream>>>(x, wqkv, w1, ws, wo, w2, w3, w4);
  k_phase2<<<256, 512, LDS_TOTAL, stream>>>(x, wqkv, wo, w2, w4, ws);
  k_phase3<<<256, 512, LDS_TOTAL, stream>>>(w3, ws);
  k_phase4<<<256, 512, LDS_TOTAL, stream>>>(x, ws, (char*)d_out);
}
```

```cpp
#include <hip/hip_runtime.h>
#include <stdint.h>

typedef __attribute__((ext_vector_type(8))) __bf16 bf16x8;
typedef __attribute__((ext_vector_type(2))) __bf16 bf16x2;
typedef __attribute__((ext_vector_type(4))) float f32x4;
typedef __attribute__((ext_vector_type(2))) float f32x2;
typedef __attribute__((ext_vector_type(4))) short v4i16;
typedef __attribute__((ext_vector_type(8))) short v8i16;
typedef __attribute__((ext_vector_type(4))) unsigned u32x4;
typedef __attribute__((ext_vector_type(2))) unsigned u32x2;
#define LAS __attribute__((address_space(3)))
#define DI __device__ __forceinline__
#define SB() __builtin_amdgcn_sched_barrier(0)

constexpr int S_ = 2048, D_ = 640, F_ = 1536;
constexpr size_t KB = 1024;
constexpr size_t OFF_PK = 0;
constexpr size_t OFF_V = OFF_PK + 64 * 40 * KB;
constexpr size_t OFF_H = OFF_V + 64 * 40 * KB;
constexpr size_t OFF_X1 = OFF_H + 128 * 20 * KB;
constexpr size_t OFF_H3 = OFF_X1 + 128 * 20 * KB;
constexpr size_t OFF_PQ = OFF_H3 + 128 * 48 * KB;
constexpr size_t OFF_SA = OFF_PQ + 128 * 20 * KB;
constexpr size_t OFF_W4 = OFF_SA + 128 * 20 * KB;
constexpr size_t OFF_ME = OFF_W4 + 48 * 40 * KB;
constexpr size_t OFF_ZP = OFF_ME + 20 * 40 * KB;
constexpr size_t OFF_ST = OFF_ZP + 16 * 640 * 4;
constexpr size_t OFF_XS = OFF_ST + 2048 * 8;
constexpr float EPS = 1e-5f;
#define STAMP(k, i)
#define STAMPEND(k)
#define CSTAMP(k, i)

DI void dma16(const void* sbase, unsigned voff, unsigned ldsdst) {
  unsigned keep;
  asm volatile("s_mov_b32 %0, m0\n\ts_mov_b32 m0, %3\n\ts_nop 0\n\tglobal_load_lds_dwordx4 %1, %2\n\ts_mov_b32 m0, %0"
               : "=&s"(keep) : "v"(voff), "s"(sbase), "s"(ldsdst) : "memory");
}
DI void dma4(const void* sbase, unsigned voff, unsigned ldsdst) {
  unsigned keep;
  asm volatile("s_mov_b32 %0, m0\n\ts_mov_b32 m0, %3\n\ts_nop 0\n\tglobal_load_lds_dword %1, %2\n\ts_mov_b32 m0, %0"
               : "=&s"(keep) : "v"(voff), "s"(sbase), "s"(ldsdst) : "memory");
}
#define CODE_ADDR(sym, out) asm volatile("s_getpc_b64 s[40:41]\n\ts_add_u32 s40, s40, " sym "@rel32@lo+4\n\ts_addc_u32 s41, s41, " sym "@rel32@hi+12\n\ts_mov_b64 %0, s[40:41]" \
                                         : "=s"(out) :: "s40", "s41", "scc")
#define T1 0
#define CSTR 64
#define L2T 0
constexpr unsigned P4LEN = 9088;
constexpr int XC = 1;
DI void touch_code(unsigned gid, unsigned lane, unsigned ldsdst) {
  constexpr int dummy = 0; (void)dummy;
  unsigned long long a, b;
  CODE_ADDR("k_phase1", a); CODE_ADDR("k_phase4", b);
  const unsigned len = min((unsigned)(b - a) + P4LEN, 131072u), o = (gid * 64 + lane) * CSTR;
  dma4((const char*)a, min(o, len - 128), ldsdst);
}
DI void st16(void* p, u32x4 v) { *(u32x4*)p = v; }
DI void st16wt(void* p, u32x4 v) { asm volatile("global_store_dwordx4 %0, %1, off sc1\n\ts_nop 1" ::"v"(p), "v"(v) : "memory"); }
DI float dot2(unsigned a, unsigned b, float c) { return __builtin_amdgcn_fdot2_f32_bf16(__builtin_bit_cast(bf16x2, a), __builtin_bit_cast(bf16x2, b), c, false); }
#define BAR() asm volatile("s_waitcnt lgkmcnt(0)\n\ts_barrier" ::: "memory")
template <int N> DI void waitvm() { asm volatile("s_waitcnt vmcnt(%0)" ::"n"(N) : "memory"); }
DI unsigned pk2(float lo, float hi) { f32x2 v = {lo, hi}; bf16x2 b = __builtin_convertvector(v, bf16x2); return __builtin_bit_cast(unsigned, b); }
DI unsigned pk2v(f32x2 v) { bf16x2 b = __builtin_convertvector(v, bf16x2); return __builtin_bit_cast(unsigned, b); }
DI float bflo(unsigned u) { return __uint_as_float(u << 16); }
DI float bfhi(unsigned u) { return __uint_as_float(u & 0xffff0000u); }
DI f32x2 unpk(unsigned u) { return f32x2{bflo(u), bfhi(u)}; }
DI float ex2(float x) { return __builtin_amdgcn_exp2f(x); }
DI float rcp(float x) { return __builtin_amdgcn_rcpf(x); }
DI f32x2 lo2(f32x4 v) { return f32x2{v[0], v[1]}; }
DI f32x2 hi2(f32x4 v) { return f32x2{v[2], v[3]}; }
DI f32x2 gelu2(f32x2 t) {
  const float c1 = -1.5957691216057308f * 1.4426950408889634f, c2 = c1 * 0.044715f;
  f32x2 u = t * t;
  f32x2 z = t * (u * c2 + c1);
  f32x2 d = f32x2{ex2(z[0]), ex2(z[1])} + 1.0f;
  return t * f32x2{rcp(d[0]), rcp(d[1])};
}
DI f32x2 phi2(f32x2 t) {
  f32x2 a = t + (1.0f + 1e-6f), z = t * 1.4426950408889634f;
  f32x2 e = f32x2{ex2(z[0]), ex2(z[1])} + 1e-6f;
  return f32x2{t[0] > 0.f ? a[0] : e[0], t[1] > 0.f ? a[1] : e[1]};
}
DI float shx(float v, int m) { return __shfl_xor(v, m, 64); }
DI bf16x8 lds128(LAS const char* p) { return *(LAS const bf16x8*)p; }
DI v4i16 ldstr(LAS const char* p) { return __builtin_amdgcn_ds_read_tr16_b64_v4i16((LAS v4i16*)p); }
DI bf16x8 cat(v4i16 a, v4i16 b) { v8i16 r = __builtin_shufflevector(a, b, 0, 1, 2, 3, 4, 5, 6, 7); return __builtin_bit_cast(bf16x8, r); }
DI f32x4 mfma(bf16x8 a, bf16x8 b, f32x4 c) { return __builtin_amdgcn_mfma_f32_16x16x32_bf16(a, b, c, 0, 0, 0); }

template <int MB_, int NB_, int NB0_, int KS_, int D_R, bool XROW_, bool FRAGW_>
struct Cfg {
  static constexpr int MB = MB_, NB = NB_, NB0 = NB0_, KS = KS_, D = D_R;
  static constexpr bool XROW = XROW_, FRAGW = FRAGW_;
  static constexpr int MBW = MB / 2, RB = NB * 32, TOKB = MB * KS * 1024, WB = KS * 32 * RB, STAGE = TOKB + WB;
  static constexpr int PT = TOKB / 4096, PW = WB / 4096, P = PT + PW;
};
typedef Cfg<8, 8, 4, 2, 4, true, false> CfgA1;
typedef Cfg<8, 8, 4, 2, 4, false, false> CfgA;
typedef Cfg<4, 10, 5, 2, 4, true, false> CfgQ;
typedef Cfg<8, 10, 5, 2, 4, false, true> CfgS;
typedef Cfg<4, 5, 3, 4, 4, false, true> CfgC;
constexpr int LDS_AUX = 147456;
constexpr int LDS_TOTAL = LDS_AUX + 4096;
#define RLX __ATOMIC_RELAXED, __HIP_MEMORY_SCOPE_AGENT

template <int RB> DI unsigned wswz(unsigned rho) { return RB == 256 ? 2 * (rho & 7) : (RB == 320 ? 2 * ((rho >> 2) & 1) : 0); }

template <class C, int XT, int XE, class Pre, class Post, class Ext, class Fin>
DI void loader(int w, unsigned lds0, const char* tokbase, int tokstride, const unsigned (&tokv)[C::PT],
               const char* wb, int wstride, const unsigned (&wv)[C::PW], int NS, Pre pre, Post post, Ext ext, Fin fin) {
  static_assert(C::D == 4, "ring depth");
  auto issue = [&](int s) {
    const char* tb = tokbase + (size_t)s * tokstride;
    const char* wp = wb + (size_t)s * wstride;
    unsigned dst = lds0 + (s & 3) * C::STAGE + w * 1024;
#pragma unroll
    for (int i = 0; i < C::PT; i++) dma16(tb, tokv[i], dst + i * 4096);
#pragma unroll
    for (int i = 0; i < C::PW; i++) dma16(wp, wv[i], dst + C::TOKB + i * 4096);
  };
  issue(0); issue(1); pre();
  waitvm<C::P + XT>(); BAR();
  issue(2); issue(3); post(0);
  waitvm<2 * C::P + XT>(); BAR();
  issue(4); post(1);
#pragma unroll 1
  for (int j = 2; j < NS - 3; j++) {
    waitvm<2 * C::P>(); BAR();
    issue(j + 3); post(j);
  }
  waitvm<2 * C::P>(); BAR();
  ext(lds0 + (NS & 3) * C::STAGE); post(NS - 3);
  waitvm<C::P + XE>(); BAR();
  post(NS - 2);
  waitvm<XE>(); BAR();
  post(NS - 1);
  fin();
}
struct Nop { DI void operator()() const {} DI void operator()(int) const {} DI void operator()(unsigned) const {} };

template <class C> DI void make_wv(unsigned (&wv)[C::PW], unsigned w, unsigned lane, unsigned pitch) {
#pragma unroll
  for (int i = 0; i < C::PW; i++) {
    if (C::FRAGW) {
      unsigned pc = i * 4 + w, ks = pc / C::NB, nb = pc % C::NB;
      wv[i] = (ks * pitch + nb) * 1024 + lane * 16;
    } else {
      unsigned sidx = (i * 4 + w) * 64 + lane;
      constexpr unsigned SL = C::RB / 16;
      unsigned rho = sidx / SL, c = sidx % SL;
      unsigned k = C::XROW ? ((rho & ~31u) | (((rho >> 2) & 3) << 3) | (((rho >> 4) & 1) << 2) | (rho & 3)) : rho;
      wv[i] = k * pitch + ((c ^ wswz<C::RB>(rho)) << 4);
    }
  }
}
template <class C> DI void make_tokv(unsigned (&tv)[C::PT], unsigned w, unsigned lane, unsigned kstot) {
#pragma unroll
  for (int i = 0; i < C::PT; i++) {
    unsigned qd = i * 4 + w;
    if (C::XROW) { unsigned row = qd * 8 + (lane >> 3); tv[i] = row * 1280 + (((lane & 7) ^ ((row >> 1) & 7)) << 4); }
    else { unsigned mb = qd / C::KS, ks = qd % C::KS; tv[i] = (mb * kstot + ks) * 1024 + lane * 16; }
  }
}

template <class C, int NBW> struct Frags { bf16x8 t[C::MBW]; v4i16 wl[NBW], wh[NBW]; bf16x8 wf[NBW]; };
template <class C> struct CAddr { int tok[2]; int tokb[2]; int wa[4]; };
template <class C> DI void make_caddr(CAddr<C>& a, int wm, int wn, int lane, int pi = 0, int pj = 0) {
  const int g = lane >> 4, q = (lane >> 2) & 3, p = lane & 3, t = lane & 15;
  if (C::XROW) {
    a.tok[0] = (wm * C::MBW * 16 + t) * 128 + ((g ^ ((t >> 1) & 7)) << 4);
    a.tok[1] = (wm * C::MBW * 16 + t) * 128 + (((4 + g) ^ ((t >> 1) & 7)) << 4);
  } else { a.tok[0] = wm * C::MBW * C::KS * 1024 + lane * 16; a.tok[1] = 0; }
  {
    const int rot = pi * (C::XROW ? 2048 : C::KS * 1024);
    a.tokb[0] = a.tok[0] - rot; a.tokb[1] = a.tok[1] - rot; a.tok[0] += rot; a.tok[1] += rot;
  }
  int r0 = 4 * g + q, base = C::TOKB + r0 * C::RB + (p >> 1) * 16 + (p & 1) * 8;
  if (C::FRAGW) { a.wa[0] = C::TOKB + wn * C::NB0 * 1024 + lane * 16; a.wa[1] = a.wa[2] = a.wa[3] = 0; }
  else if (C::RB == 256) {
#pragma unroll
    for (int j = 0; j < 4; j++) a.wa[j] = base + (((wn * 4 + ((j + pj) & 3)) ^ (r0 & 7)) << 5);
  } else {
    int b = g & 1, n0 = wn * C::NB0;
    a.wa[0] = base + ((n0 ^ b) << 5);
    a.wa[1] = base + (((n0 + 1) ^ b) << 5) - 32;
    a.wa[2] = a.wa[3] = 0;
  }
}
template <class C, int NBW> struct RD { static constexpr int WR = C::FRAGW ? 1 : 2, NR = C::MBW + WR * NBW; };
template <class C, int NBW>
DI void read_one(Frags<C, NBW>& f, LAS const char* slot, const CAddr<C>& a, int ks, int r) {
  constexpr int WR = RD<C, NBW>::WR;
  int ti = -1, wj = -1, h = 0;
  if (r == 0) ti = 0;
  else if (r <= WR) { wj = 0; h = r - 1; }
  else if (r < WR + C::MBW) ti = r - WR;
  else { int q = r - C::MBW; wj = q / WR; h = q % WR; }
  if (ti >= 0) {
    const bool hi = ti >= C::MBW / 2;
    f.t[ti] = C::XROW ? lds128(slot + (hi ? a.tokb[ks & 1] : a.tok[ks & 1]) + ti * 2048)
                      : lds128(slot + (hi ? a.tokb[0] : a.tok[0]) + (ti * C::KS + ks) * 1024);
  } else if (C::FRAGW) {
    f.wf[wj] = lds128(slot + a.wa[0] + (ks * C::NB + wj) * 1024);
  } else {
    LAS const char* pw = C::RB == 256 ? slot + a.wa[wj] : slot + a.wa[wj & 1] + wj * 32;
    if (h == 0) f.wl[wj] = ldstr(pw + ks * 32 * C::RB);
    else f.wh[wj] = ldstr(pw + (ks * 32 + 16) * C::RB);
  }
}
template <class C, int NBW> DI bf16x8 wfrag(const Frags<C, NBW>& f, int j) { return C::FRAGW ? f.wf[j] : cat(f.wl[j], f.wh[j]); }
template <class C, int NBW>
DI void load_all(Frags<C, NBW>& f, LAS const char* slot, const CAddr<C>& a, int ks) {
#pragma unroll
  for (int r = 0; r < RD<C, NBW>::NR; r++) read_one<C, NBW>(f, slot, a, ks, r);
}
template <int NCS_, int CSM_, int NTS_> struct Extra {
  static constexpr int NCS = NCS_, CSM = CSM_, NTS = NTS_;
  f32x4 cs[NCS_ ? NCS_ : 1], ts[NTS_ ? NTS_ : 1], tq[NTS_ ? NTS_ : 1];
};
template <class C, int NBW, bool SWAP, bool BARR, bool RDN, class E>
DI void kstep(f32x4 (&acc)[C::MBW][NBW], E& ex, const Frags<C, NBW>& cur, Frags<C, NBW>& nxt,
              LAS const char* nslot, const CAddr<C>& a, int nks, bf16x8 ones) {
  constexpr int MBW = C::MBW, NM = MBW * NBW, NR = RD<C, NBW>::NR;
  constexpr int F0 = BARR ? MBW : 0, NSL = NM - F0, PER = (NR + NSL - 1) / NSL;
#pragma unroll
  for (int m = 0; m < NM; m++) {
    const int j = m / MBW, i = m % MBW;
    bf16x8 wf = wfrag<C, NBW>(cur, j);
    acc[i][j] = SWAP ? mfma(wf, cur.t[i], acc[i][j]) : mfma(cur.t[i], wf, acc[i][j]);
    if (i == MBW - 1 && j < E::NCS) ex.cs[j] = E::CSM ? mfma(wf, ones, ex.cs[j]) : mfma(ones, wf, ex.cs[j]);
    if (j == 2 && i < E::NTS) ex.ts[i] = mfma(ones, cur.t[i], ex.ts[i]);
    if (j == 3 && i < E::NTS) ex.tq[i] = mfma(cur.t[i], cur.t[i], ex.tq[i]);
    if (BARR && m == F0 - 1) { SB(); BAR(); }
    if (RDN && m >= F0) {
#pragma unroll
      for (int r = (m - F0) * PER; r < (m - F0 + 1) * PER && r < NR; r++) read_one<C, NBW>(nxt, nslot, a, nks, r);
    }
    SB();
  }
}
template <class C, int NBW, bool SWAP, bool LAST, class E>
DI void consume(f32x4 (&acc)[C::MBW][NBW], E& ex, Frags<C, NBW>& fa, LAS const char* lds, const CAddr<C>& a, int s0, int s1, bf16x8 ones) {
  Frags<C, NBW> fb;
  const int e = LAST ? s1 - 1 : s1;
#pragma unroll 1
  for (int s = s0; s < e; s++) {
    LAS const char* slot = lds + (s & 3) * C::STAGE;
    LAS const char* nsl = lds + ((s + 1) & 3) * C::STAGE;
#pragma unroll
    for (int ks = 0; ks < C::KS; ks += 2) {
      kstep<C, NBW, SWAP, false, true>(acc, ex, fa, fb, slot, a, ks + 1, ones);
      if (ks + 2 < C::KS) kstep<C, NBW, SWAP, false, true>(acc, ex, fb, fa, slot, a, ks + 2, ones);
      else kstep<C, NBW, SWAP, true, true>(acc, ex, fb, fa, nsl, a, 0, ones);
    }
  }
  if (LAST) {
    LAS const char* slot = lds + (e & 3) * C::STAGE;
#pragma unroll
    for (int ks = 0; ks < C::KS; ks += 2) {
      kstep<C, NBW, SWAP, false, true>(acc, ex, fa, fb, slot, a, ks + 1, ones);
      if (ks + 2 < C::KS) kstep<C, NBW, SWAP, false, true>(acc, ex, fb, fa, slot, a, ks + 2, ones);
      else kstep<C, NBW, SWAP, false, false>(acc, ex, fb, fa, slot, a, 0, ones);
    }
  }
}
template <int A, int B> DI void zero(f32x4 (&acc)[A][B]) {
#pragma unroll
  for (int i = 0; i < A; i++)
#pragma unroll
    for (int j = 0; j < B; j++) acc[i][j] = f32x4{0.f, 0.f, 0.f, 0.f};
}
template <int A, int M, int B> DI void zero(Extra<A, M, B>& e) {
#pragma unroll
  for (int i = 0; i < (A ? A : 1); i++) e.cs[i] = f32x4{0.f, 0.f, 0.f, 0.f};
#pragma unroll
  for (int i = 0; i < (B ? B : 1); i++) e.ts[i] = e.tq[i] = f32x4{0.f, 0.f, 0.f, 0.f};
}
DI bf16x8 ones_frag() { u32x4 o = {0x3f803f80u, 0x3f803f80u, 0x3f803f80u, 0x3f803f80u}; return __builtin_bit_cast(bf16x8, o); }
#define CBAR() asm volatile("s_waitcnt lgkmcnt(0)\n\ts_barrier" ::: "memory")

extern __shared__ __attribute__((aligned(16))) char smem[];
#define AUXF ((LAS float*)(lds + LDS_AUX))
DI f32x4 ldsf4(LAS const float* p) { return *(LAS const f32x4*)p; }
DI float sel4(f32x4 v, int k) { float a = (k & 1) ? v[1] : v[0], b = (k & 1) ? v[3] : v[2]; return (k & 2) ? b : a; }

extern "C" __global__ __launch_bounds__(512) void k_phase1(const char* __restrict__ x, const char* __restrict__ wqkv, const char* __restrict__ w1, char* __restrict__ ws,
                                              const char* __restrict__ wo, const char* __restrict__ w2, const char* __restrict__ w3, const char* __restrict__ w4) {
  typedef CfgA1 C;
  const int b = blockIdx.x, xcd = b & 7, idx = b >> 3;
  const int rt = (xcd >> 1) * 4 + (idx & 3), ct = (xcd & 1) * 8 + (idx >> 2);
  const int wid = __builtin_amdgcn_readfirstlane(threadIdx.x >> 6), lane = threadIdx.x & 63;
  LAS char* lds = (LAS char*)smem;
  STAMP(0, 0);
  if (ct >= 15) return;
  if (wid >= 4) {
    const int w = wid - 4;
    unsigned tv[C::PT], wv[C::PW];
    const char* wb = ct < 10 ? wqkv + (640 + ct * 128) * 2 : w1 + (ct - 10) * 256;
    const int pitch = ct < 10 ? 3840 : 1280;
    make_tokv<C>(tv, w, lane, 0); make_wv<C>(wv, w, lane, pitch);
    float s[4] = {0.f, 0.f, 0.f, 0.f}, q[4] = {0.f, 0.f, 0.f, 0.f};
    LAS const char* my = lds + w * 1024 + lane * 16;
    auto post = [&](int j) {
      LAS const char* p = my + (j & 3) * C::STAGE;
#pragma unroll
      for (int i = 0; i < 4; i++) {
        u32x4 v = *(LAS const u32x4*)(p + i * 4096);
#pragma unroll
        for (int e = 0; e < 4; e++) { s[i] = dot2(v[e], 0x3f803f80u, s[i]); q[i] = dot2(v[e], v[e], q[i]); }
      }
    };
    auto fin = [&]() {
#pragma unroll
      for (int i = 0; i < 4; i++) {
#pragma unroll
        for (int m = 1; m < 8; m <<= 1) { s[i] += shx(s[i], m); q[i] += shx(q[i], m); }
      }
      const int i = lane & 3;
      const float sv = sel4(f32x4{s[0], s[1], s[2], s[3]}, i), qv = sel4(f32x4{q[0], q[1], q[2], q[3]}, i);
      const float m = sv * (1.0f / D_), var = fmaxf(qv * (1.0f / D_) - m * m, 0.f);
      const float rs = __builtin_amdgcn_rsqf(var + EPS);
      float r = rs;
      if (ct >= 10) r = rs * __builtin_amdgcn_rsqf(var * rs * rs + EPS);
      const int row = (i * 4 + w) * 8 + (lane >> 3);
      if ((lane & 7) < 4) {
        AUXF[row] = r; AUXF[128 + row] = r * m;
        if (ct == 0) ((f32x2*)(ws + OFF_ST))[rt * 128 + row] = f32x2{m, rs};
      }
      BAR();
    };
    const unsigned junk = (unsigned)(size_t)lds + LDS_AUX + 3584;
    auto pre = [&]() {
      if (T1) {
        const unsigned gid = min(b * 4 + w, 719);
        const char* p1 = gid < 320 ? x + gid * 8192 : (gid < 620 ? wqkv + (gid - 320) * 8192 : w1 + (gid - 620) * 8192);
        dma4(p1, lane * 128, junk);
      }
      touch_code(idx * 4 + w, lane, junk);
    };
    loader<C, T1 + XC, 0>(w, (unsigned)(size_t)lds, x + (size_t)rt * 128 * 1280, 128, tv, wb, 64 * pitch, wv, 10, pre, post, Nop(), fin);
    waitvm<0>();
    STAMP(0, 1);
    return;
  }
  const int wm = wid >> 1, wn = wid & 1, g = lane >> 4, tk = lane & 15;
  CAddr<C> a; make_caddr<C>(a, wm, wn, lane, 0, 2 * wm);
  f32x4 acc[4][4]; zero(acc);
  Extra<2, 0, 0> ex; zero(ex);
  const bf16x8 ones = ones_frag();
  Frags<C, 4> fa;
  CBAR(); STAMP(0, 1); CSTAMP(0, 6); load_all<C, 4>(fa, lds, a, 0);
  if (ct < 10) consume<C, 4, false, true>(acc, ex, fa, lds, a, 0, 10, ones);
  else consume<C, 4, true, true>(acc, ex, fa, lds, a, 0, 10, ones);
  STAMP(0, 2); CSTAMP(0, 7);
  {
    const unsigned gid = b * 4 + wid, gq = gid * 64 + lane;
    const char* p2 = gid < 100 ? wqkv : (gid < 200 ? wo - 100 * 8192 : (gid < 300 ? w2 - 200 * 8192 : (gid < 540 ? w3 - 300 * 8192 : w4 - 540 * 8192)));
    if (gid < 780) dma4(p2, gid < 100 ? (gq / 10) * 3840 + (gq % 10) * 128 : gq * 128, (unsigned)(size_t)lds + LDS_AUX + 3584);
  }
  if (g == 0) {
#pragma unroll
    for (int i = 0; i < 2; i++) AUXF[256 + wn * 64 + (2 * wm + i) * 16 + tk] = ex.cs[i][0];
  }
  CBAR();
  STAMP(0, 3);
  if (ct < 10) {
    char* dst = ws + (ct < 5 ? OFF_PK : OFF_V) + lane * 16;
    const int cb0 = (ct < 5 ? ct : ct - 5) * 8 + wn * 4;
    float cs[4]; f32x2 zs[4];
#pragma unroll
    for (int j = 0; j < 4; j++) { cs[j] = AUXF[256 + wn * 64 + ((j + 2 * wm) & 3) * 16 + tk]; zs[j] = f32x2{0.f, 0.f}; }
#pragma unroll
    for (int ip = 0; ip < 2; ip++) {
      const int tb = wm * 64 + ip * 32 + 4 * g;
      f32x4 r0 = ldsf4(AUXF + tb), r1 = ldsf4(AUXF + tb + 16), m0 = ldsf4(AUXF + 128 + tb), m1 = ldsf4(AUXF + 128 + tb + 16);
      char* d2 = dst + (size_t)((rt * 4 + wm * 2 + ip) * 40 + cb0) * 1024;
#pragma unroll
      for (int j = 0; j < 4; j++) {
        f32x4 t0 = r0 * acc[2 * ip][j] - m0 * cs[j], t1 = r1 * acc[2 * ip + 1][j] - m1 * cs[j];
        u32x4 o;
        if (ct < 5) {
          o = u32x4{pk2v(phi2(lo2(t0))), pk2v(phi2(hi2(t0))), pk2v(phi2(lo2(t1))), pk2v(phi2(hi2(t1)))};
          zs[j] += (unpk(o[0]) + unpk(o[1])) + (unpk(o[2]) + unpk(o[3]));
        } else {
          o = u32x4{pk2v(lo2(t0)), pk2v(hi2(t0)), pk2v(lo2(t1)), pk2v(hi2(t1))};
        }
        st16(d2 + ((j + 2 * wm) & 3) * 1024, o);
      }
    }
    if (ct < 5) {
      float z[4];
#pragma unroll
      for (int j = 0; j < 4; j++) { z[j] = zs[j][0] + zs[j][1]; z[j] += shx(z[j], 16); z[j] += shx(z[j], 32); }
      if (wm == 1 && lane < 16) {
#pragma unroll
        for (int j = 0; j < 4; j++) AUXF[704 + wn * 64 + ((j + 2) & 3) * 16 + lane] = z[j];
      }
      CBAR();
      if (wm == 0 && lane < 16) {
        float* zp = (float*)(ws + OFF_ZP) + rt * 640 + ct * 128 + wn * 64;
#pragma unroll
        for (int j = 0; j < 4; j++) zp[j * 16 + lane] = z[j] + AUXF[704 + wn * 64 + j * 16 + lane];
      }
    }
  } else {
    const int c0 = (ct - 10) * 128 + wn * 64;
#pragma unroll
    for (int i = 0; i < 4; i++) {
      const int row = wm * 64 + i * 16 + tk;
      const float rr = AUXF[row], rm = AUXF[128 + row];
#pragma unroll
      for (int jp = 0; jp < 2; jp++) {
        const int jt = jp ^ wm;
        u32x4 o;
#pragma unroll
        for (int h = 0; h < 2; h++) {
          f32x4 cs = ldsf4(AUXF + 256 + wn * 64 + (2 * jt + h) * 16 + 4 * g);
          f32x4 t = rr * acc[i][2 * jp + h] - rm * cs;
          o[2 * h] = pk2v(gelu2(lo2(t))); o[2 * h + 1] = pk2v(gelu2(hi2(t)));
        }
        const int col = c0 + jt * 32, grp = col / 160;
        const int dl = grp == 0 ? 1 : (grp == 1 ? -1 : (grp == 2 ? 2 : -2));
        int td = rt * 128 + row + dl;
        const bool ok = td >= 0 && td < S_;
        td &= S_ - 1;
        if (!ok) o = u32x4{0, 0, 0, 0};
        st16(ws + OFF_H + ((size_t)((td >> 4) * 20 + (col >> 5)) * 64 + g * 16 + (td & 15)) * 16, o);
      }
    }
  }
  waitvm<0>();
  STAMPEND(0);
}

extern "C" __global__ __launch_bounds__(512) void k_phase2(const char* __restrict__ x, const char* __restrict__ wqkv, const char* __restrict__ wo, const char* __restrict__ w2,
                                              const char* __restrict__ w4, char* __restrict__ ws) {
  const int b = blockIdx.x;
  const int wid = __builtin_amdgcn_readfirstlane(threadIdx.x >> 6), lane = threadIdx.x & 63;
  LAS char* lds = (LAS char*)smem;
  const int g = lane >> 4, tk = lane & 15;
  STAMP(1, 0);
  if (b < 128) {
    typedef CfgQ C;
    const int rt = b >> 2, ct = b & 3;
    if (wid >= 4) {
      const int w = wid - 4;
      unsigned tv[C::PT], wv[C::PW];
      make_tokv<C>(tv, w, lane, 0); make_wv<C>(wv, w, lane, 3840);
      loader<C, XC, 0>(w, (unsigned)(size_t)lds, x + (size_t)rt * 64 * 1280, 128, tv, wqkv + ct * 320, 64 * 3840, wv, 10, [&]() { touch_code((b >> 3) * 4 + w, lane, (unsigned)(size_t)lds + LDS_AUX + 3584); }, Nop(), Nop(), Nop());
      STAMP(1, 1);
      return;
    }
    const int wm = wid >> 1, wn = wid & 1;
    float zp[16];
    const int t = threadIdx.x;
    if (t < 160) {
      const float* p = (const float*)(ws + OFF_ZP) + ct * 160 + t;
#pragma unroll
      for (int r = 0; r < 16; r++) zp[r] = p[r * 640];
    }
    f32x2 st[2];
#pragma unroll
    for (int i = 0; i < 2; i++) st[i] = ((const f32x2*)(ws + OFF_ST))[rt * 64 + wm * 32 + i * 16 + tk];
    CAddr<C> a; make_caddr<C>(a, wm, wn, lane);
    f32x4 acc[2][5]; zero(acc);
    Extra<5, 1, 0> ex; zero(ex);
    const bf16x8 ones = ones_frag();
    Frags<C, 5> fa;
    CBAR(); STAMP(1, 1); CSTAMP(1, 6); load_all<C, 5>(fa, lds, a, 0);
    if (t < 160) {
      float z = 0.f;
#pragma unroll
      for (int r = 0; r < 16; r += 4) z += (zp[r] + zp[r + 1]) + (zp[r + 2] + zp[r + 3]);
      AUXF[512 + t] = z;
    }
    consume<C, 5, true, true>(acc, ex, fa, lds, a, 0, 10, ones);
    STAMP(1, 2); CSTAMP(1, 7);
#pragma unroll
    for (int i = 0; i < 2; i++) {
      const float rs = st[i][1], rm = rs * st[i][0];
      f32x4 pq[5]; float part[5];
#pragma unroll
      for (int j = 0; j < 5; j++) {
        f32x4 z = ldsf4(AUXF + 512 + wn * 80 + j * 16 + 4 * g);
        f32x4 tt = rs * acc[i][j] - rm * ex.cs[j];
        f32x2 p0 = phi2(lo2(tt)), p1 = phi2(hi2(tt));
        pq[j] = f32x4{p0[0], p0[1], p1[0], p1[1]};
        f32x2 s = p0 * lo2(z) + p1 * hi2(z);
        part[j] = s[0] + s[1];
      }
      float dh[4];
      dh[0] = part[0] + (g == 0 ? part[1] : 0.f);
      dh[1] = (g >= 1 ? part[1] : 0.f) + (g <= 1 ? part[2] : 0.f);
      dh[2] = (g >= 2 ? part[2] : 0.f) + (g <= 2 ? part[3] : 0.f);
      dh[3] = (g == 3 ? part[3] : 0.f) + part[4];
#pragma unroll
      for (int h = 0; h < 4; h++) { dh[h] += shx(dh[h], 16); dh[h] += shx(dh[h], 32); dh[h] = rcp(dh[h] + 1e-6f); }
      const float sc[5] = {dh[0], g == 0 ? dh[0] : dh[1], g <= 1 ? dh[1] : dh[2], g <= 2 ? dh[2] : dh[3], dh[3]};
#pragma unroll
      for (int j = 0; j < 5; j++) pq[j] *= sc[j];
      char* dst = ws + OFF_PQ + ((size_t)(rt * 4 + wm * 2 + i) * 20 + ct * 5) * 1024 + lane * 16;
      st16(dst + (3 * wn) * 1024, u32x4{pk2v(lo2(pq[0])), pk2v(hi2(pq[0])), pk2v(lo2(pq[1])), pk2v(hi2(pq[1]))});
      st16(dst + (3 * wn + 1) * 1024, u32x4{pk2v(lo2(pq[2])), pk2v(hi2(pq[2])), pk2v(lo2(pq[3])), pk2v(hi2(pq[3]))});
      *(u32x2*)(dst + 2 * 1024 + wn * 8) = u32x2{pk2v(lo2(pq[4])), pk2v(hi2(pq[4]))};
    }
    STAMPEND(1);
  } else if (b < 208) {
    typedef CfgA C;
    const int bb = b - 128, rt = (bb & 7) * 2 + (bb >> 3) / 5, ct = (bb >> 3) % 5;
    if (wid >= 4) {
      const int w = wid - 4;
      unsigned tv[C::PT], wv[C::PW];
      make_tokv<C>(tv, w, lane, 20); make_wv<C>(wv, w, lane, 1280);
      const char* xb = x + (size_t)rt * 128 * 1280 + ct * 256;
      const unsigned xr = w * 4 + g, xo = xr * 1280 + ((tk ^ xr) << 4);
      auto ext = [&](unsigned dst) {
#pragma unroll
        for (int i = 0; i < 8; i++) dma16(xb, xo + i * 16 * 1280, dst + (i * 4 + w) * 1024);
      };
      auto fin = [&]() { waitvm<0>(); BAR(); };
      loader<C, 0, 8>(w, (unsigned)(size_t)lds, ws + OFF_H + (size_t)rt * 8 * 20 * 1024, 2048, tv, w2 + ct * 256, 64 * 1280, wv, 10, Nop(), Nop(), ext, fin);
      STAMP(1, 1);
      return;
    }
    const int wm = wid >> 1, wn = wid & 1;
    f32x2 st[4];
#pragma unroll
    for (int i = 0; i < 4; i++) st[i] = ((const f32x2*)(ws + OFF_ST))[rt * 128 + wm * 64 + i * 16 + tk];
    CAddr<C> a; make_caddr<C>(a, wm, wn, lane);
    f32x4 acc[4][4]; zero(acc);
    Extra<0, 0, 0> ex;
    const bf16x8 ones = ones_frag();
    Frags<C, 4> fa;
    CBAR(); STAMP(1, 1); CSTAMP(1, 6); load_all<C, 4>(fa, lds, a, 0);
    consume<C, 4, true, true>(acc, ex, fa, lds, a, 0, 10, ones);
    STAMP(1, 2); CSTAMP(1, 7);
    CBAR();
    LAS const char* xs = lds + 2 * C::STAGE + (wm * 64 + tk) * 256 + (g & 1) * 8;
#pragma unroll
    for (int i = 0; i < 4; i++) {
      const float rs = st[i][1], rm = st[i][0] * rs;
      f32x2 s1 = {0.f, 0.f}, s2 = {0.f, 0.f};
      char* dst = ws + OFF_X1 + ((size_t)(rt * 8 + wm * 4 + i) * 20 + ct * 4 + wn * 2) * 1024 + lane * 16;
#pragma unroll
      for (int jp = 0; jp < 2; jp++) {
        u32x4 o;
#pragma unroll
        for (int h = 0; h < 2; h++) {
          u32x2 xx = *(LAS const u32x2*)(xs + i * 16 * 256 + (((wn * 8 + (2 * jp + h) * 2 + (g >> 1)) ^ tk) << 4));
          f32x4 c = acc[i][2 * jp + h] - rm;
          o[2 * h] = pk2v(unpk(xx[0]) * rs + lo2(c));
          o[2 * h + 1] = pk2v(unpk(xx[1]) * rs + hi2(c));
        }
#pragma unroll
        for (int e = 0; e < 4; e++) { f32x2 v = unpk(o[e]); s1 += v; s2 += v * v; }
        st16(dst + jp * 1024, o);
      }
      float a1 = s1[0] + s1[1], a2 = s2[0] + s2[1];
      a1 += shx(a1, 16); a2 += shx(a2, 16); a1 += shx(a1, 32); a2 += shx(a2, 32);
      if (g == 0) ((f32x2*)(ws + OFF_XS))[(ct * 2 + wn) * 2048 + rt * 128 + wm * 64 + i * 16 + tk] = f32x2{a1, a2};
    }
    STAMPEND(1);
  } else if (b < 240) {
    const int h = b - 208, hh = h & 3, cb0 = 5 * (h >> 2) + hh;
    LAS char* wt = lds + 40960;
    {
      const char* wb = wo + (size_t)cb0 * 16 * 1280;
#pragma unroll
      for (int i = 0; i < 5; i++) {
        const unsigned pc = i * 8 + wid, s = pc * 64 + lane, qt = s / 640, r = (s % 640) / 20, c = s % 20;
        dma16(wb, r * 1280 + qt * 320 + ((c ^ (2 * ((r >> 2) & 1))) << 4), (unsigned)(size_t)wt + pc * 1024);
      }
    }
    bf16x8 pk[8][2], vv[8][2];
#pragma unroll
    for (int u = 0; u < 8; u++)
#pragma unroll
      for (int j = 0; j < 2; j++) {
        const size_t o = ((size_t)(wid * 8 + u) * 40 + cb0 + j) * 1024 + lane * 16;
        pk[u][j] = *(const bf16x8*)(ws + OFF_PK + o); vv[u][j] = *(const bf16x8*)(ws + OFF_V + o);
      }
    f32x4 acc[4];
#pragma unroll
    for (int i = 0; i < 4; i++) acc[i] = f32x4{0.f, 0.f, 0.f, 0.f};
#pragma unroll
    for (int u = 0; u < 8; u++)
#pragma unroll
      for (int i = 0; i < 4; i++) acc[i] = mfma(vv[u][i & 1], pk[u][i >> 1], acc[i]);
    STAMP(1, 2);
#pragma unroll
    for (int i = 0; i < 4; i++) *(LAS f32x4*)(lds + (wid * 4 + i) * 1024 + lane * 16) = acc[i];
    waitvm<0>();
    BAR();
    if (wid < 4) {
      f32x4 s = *(LAS const f32x4*)(lds + wid * 1024 + lane * 16);
#pragma unroll
      for (int w = 1; w < 8; w++) s += *(LAS const f32x4*)(lds + (w * 4 + wid) * 1024 + lane * 16);
      const unsigned ee = 4 * (wid & 1) + g - hh;
      u32x2 o = u32x2{pk2v(lo2(s)), pk2v(hi2(s))};
      if (ee >= 5u) o = u32x2{0u, 0u};
      *(LAS u32x2*)(lds + 32768 + wid * 512 + lane * 8) = o;
    }
    BAR();
    STAMP(1, 3);
    {
      const int q = (lane >> 2) & 3, p = lane & 3;
      bf16x8 af[2]; char* dst[2]; bool ok[2];
#pragma unroll
      for (int db = 0; db < 2; db++) {
        const u32x2 h0 = *(LAS const u32x2*)(lds + 32768 + (db * 2) * 512 + lane * 8), h1 = *(LAS const u32x2*)(lds + 32768 + (db * 2 + 1) * 512 + lane * 8);
        af[db] = __builtin_bit_cast(bf16x8, u32x4{h0[0], h0[1], h1[0], h1[1]});
        const int beta = cb0 + db, GG = beta / 10, j = beta % 10, wn = j / 5, jj = j % 5;
        const int kap = jj == 4 ? 5 * GG + 2 : 5 * GG + 3 * wn + (jj >> 1), tt = jj == 4 ? wn : (jj & 1);
        dst[db] = ws + OFF_ME + (size_t)kap * 40 * 1024 + lane * 16 + tt * 8;
        ok[db] = (unsigned)(4 * db + g - hh) < 5u;
      }
#pragma unroll
      for (int i = 0; i < 5; i++) {
        const int nb = wid * 5 + i, qt = nb / 10, nl = nb % 10;
        LAS const char* src = wt + qt * 10240 + (4 * g + q) * 320 + ((nl ^ (g & 1)) << 5) + p * 8;
        const bf16x8 wf = cat(ldstr(src), ldstr(src + 16 * 320));
#pragma unroll
        for (int db = 0; db < 2; db++) {
          const f32x4 c = mfma(af[db], wf, f32x4{0.f, 0.f, 0.f, 0.f});
          if (ok[db]) *(u32x2*)(dst[db] + nb * 1024) = u32x2{pk2v(lo2(c)), pk2v(hi2(c))};
        }
      }
    }
    STAMPEND(1);
  } else {
    LAS char* base = lds + wid * 16384;
    const unsigned lb = (unsigned)(size_t)base;
    const int q = (lane >> 2) & 3, p = lane & 3, r0 = 4 * g + q;
    const int u = (b - 240) * 8 + wid;
    if (u < 120) {
      const int kb = u / 5, cb = u % 5;
      const char* src = w4 + kb * 64 * 1280 + cb * 256;
      char* dst = ws + OFF_W4 + (kb * 80 + cb * 8) * 1024 + lane * 16;
#pragma unroll
      for (int i = 0; i < 16; i++) {
        const unsigned rho = i * 4 + g;
        dma16(src, rho * 1280 + (((lane & 15) ^ wswz<256>(rho)) << 4), lb + i * 1024);
      }
      waitvm<0>();
      LAS const char* pw = base + r0 * 256 + p * 8;
#pragma unroll
      for (int nb = 0; nb < 8; nb++)
#pragma unroll
        for (int ks = 0; ks < 2; ks++) {
          LAS const char* pp = pw + ((nb ^ (r0 & 7)) << 5) + ks * 32 * 256;
          st16(dst + (ks * 40 + nb) * 1024, __builtin_bit_cast(u32x4, cat(ldstr(pp), ldstr(pp + 16 * 256))));
        }
    }
    STAMPEND(1);
  }
}

extern "C" __global__ __launch_bounds__(512) void k_phase3(const char* __restrict__ w3, char* __restrict__ ws) {
  const int b = blockIdx.x, xcd = b & 7;
  const int wid = __builtin_amdgcn_readfirstlane(threadIdx.x >> 6), lane = threadIdx.x & 63;
  LAS char* lds = (LAS char*)smem;
  const int wm = (wid >> 1) & 1, wn = wid & 1, g = lane >> 4, tk = lane & 15;
  STAMP(2, 0);
  if (b < 192) {
    typedef CfgA C;
    const int idx = b >> 3;
    const int rt = (xcd >> 1) * 4 + (idx & 3), ct = (xcd & 1) * 6 + (idx >> 2);
    if (wid >= 4) {
      const int w = wid - 4;
      unsigned tv[C::PT], wv[C::PW];
      make_tokv<C>(tv, w, lane, 20); make_wv<C>(wv, w, lane, 3072);
      const unsigned junk = (unsigned)(size_t)lds + LDS_AUX + 3584;
      auto pre = [&]() {
        if (L2T) {
          const unsigned t = w * 64 + lane, la = min((idx >> 2) * 171 + min(t, 170u), 1023u), lb = (idx & 3) * 256 + t;
          dma4(ws + OFF_X1 + (size_t)rt * 8 * 20 * 1024, (la >> 7) * 20480 + 4096 + (la & 127) * 128, junk);
          dma4(w3 + ct * 256, (128 + (lb >> 1)) * 3072 + (lb & 1) * 128, junk);
        }
        touch_code(idx * 4 + w, lane, junk);
      };
      loader<C, XC + 2 * L2T, 0>(w, (unsigned)(size_t)lds, ws + OFF_X1 + (size_t)rt * 8 * 20 * 1024, 2048, tv, w3 + ct * 256, 64 * 3072, wv, 10, pre, Nop(), Nop(), Nop());
      STAMP(2, 1);
      return;
    }
    f32x2 xs[10];
    if (wid < 2) {
      const f32x2* p = (const f32x2*)(ws + OFF_XS) + rt * 128 + threadIdx.x;
#pragma unroll
      for (int i = 0; i < 10; i++) xs[i] = p[i * 2048];
    }
    CAddr<C> a; make_caddr<C>(a, wm, wn, lane);
    f32x4 acc[4][4]; zero(acc);
    Extra<4, 1, 0> ex; zero(ex);
    const bf16x8 ones = ones_frag();
    Frags<C, 4> fa;
    CBAR(); STAMP(2, 1); CSTAMP(2, 6); load_all<C, 4>(fa, lds, a, 0);
    if (wid < 2) {
      f32x2 s = ((xs[0] + xs[1]) + (xs[2] + xs[3])) + ((xs[4] + xs[5]) + (xs[6] + xs[7])) + (xs[8] + xs[9]);
      float m = s[0] * (1.0f / D_), var = fmaxf(s[1] * (1.0f / D_) - m * m, 0.f);
      float rs = __builtin_amdgcn_rsqf(var + EPS);
      AUXF[threadIdx.x] = rs; AUXF[128 + threadIdx.x] = rs * m;
    }
    consume<C, 4, true, true>(acc, ex, fa, lds, a, 0, 10, ones);
    STAMP(2, 2); CSTAMP(2, 7);
#pragma unroll
    for (int i = 0; i < 4; i++) {
      const int row = wm * 64 + i * 16 + tk;
      const float rs = AUXF[row], rm = AUXF[128 + row];
      char* dst = ws + OFF_H3 + ((size_t)(rt * 8 + wm * 4 + i) * 48 + ct * 4 + wn * 2) * 1024 + lane * 16;
#pragma unroll
      for (int jp = 0; jp < 2; jp++) {
        u32x4 o;
#pragma unroll
        for (int h = 0; h < 2; h++) {
          f32x4 t = rs * acc[i][2 * jp + h] - rm * ex.cs[2 * jp + h];
          o[2 * h] = pk2v(gelu2(lo2(t))); o[2 * h + 1] = pk2v(gelu2(hi2(t)));
        }
        st16(dst + jp * 1024, o);
      }
    }
    STAMPEND(2);
  } else {
    typedef CfgS C;
    const int idx = (b - 192) >> 3;
    const int rt = (xcd >> 1) * 4 + (idx & 3), ct = (xcd & 1) * 2 + (idx >> 2);
    if (wid >= 4) {
      const int w = wid - 4;
      unsigned tv[C::PT], wv[C::PW];
      make_tokv<C>(tv, w, lane, 20); make_wv<C>(wv, w, lane, 40);
      loader<C, 0, 0>(w, (unsigned)(size_t)lds, ws + OFF_PQ + (size_t)rt * 8 * 20 * 1024, 2048, tv, ws + OFF_ME + ct * 10 * 1024, 80 * 1024, wv, 10, Nop(), Nop(), Nop(), Nop());
      STAMP(2, 1);
      return;
    }
    CAddr<C> a; make_caddr<C>(a, wm, wn, lane);
    f32x4 acc[4][5]; zero(acc);
    Extra<0, 0, 0> ex;
    const bf16x8 ones = ones_frag();
    Frags<C, 5> fa;
    CBAR(); STAMP(2, 1); CSTAMP(2, 6); load_all<C, 5>(fa, lds, a, 0);
    consume<C, 5, true, true>(acc, ex, fa, lds, a, 0, 10, ones);
    STAMP(2, 2); CSTAMP(2, 7);
#pragma unroll
    for (int i = 0; i < 4; i++) {
      char* dst = ws + OFF_SA + ((size_t)(rt * 8 + wm * 4 + i) * 20 + ct * 5) * 1024 + lane * 16;
      u32x2 o[5];
#pragma unroll
      for (int j = 0; j < 5; j++) o[j] = u32x2{pk2v(lo2(acc[i][j])), pk2v(hi2(acc[i][j]))};
      if (wn == 0) {
        st16(dst, u32x4{o[0][0], o[0][1], o[1][0], o[1][1]});
        st16(dst + 1024, u32x4{o[2][0], o[2][1], o[3][0], o[3][1]});
        *(u32x2*)(dst + 2048) = o[4];
      } else {
        *(u32x2*)(dst + 2048 + 8) = o[0];
        st16(dst + 3072, u32x4{o[1][0], o[1][1], o[2][0], o[2][1]});
        st16(dst + 4096, u32x4{o[3][0], o[3][1], o[4][0], o[4][1]});
      }
    }
    STAMPEND(2);
  }
}

template <int NBW>
DI void p4_consumer(char* __restrict__ out, char* __restrict__ ws, LAS char* lds, int rt, int ct, int wm, int wn, int lane) {
  typedef CfgC C;
  const int g = lane >> 4, tk = lane & 15;
  u32x2 sav[2][NBW], x1v[2][NBW];
  CAddr<C> a; make_caddr<C>(a, wm, wn, lane);
  f32x4 h2[2][NBW]; zero(h2);
  Extra<0, 0, 0> ex;
  const bf16x8 ones = ones_frag();
  Frags<C, NBW> fa;
  CBAR(); STAMP(3, 1); CSTAMP(3, 6); load_all<C, NBW>(fa, lds, a, 0);
  consume<C, NBW, true, false>(h2, ex, fa, lds, a, 0, 9, ones);
#pragma unroll
  for (int i = 0; i < 2; i++)
#pragma unroll
    for (int j = 0; j < NBW; j++) {
      const int blk = ct * 5 + wn * 3 + j;
      const size_t o = ((size_t)(rt * 4 + wm * 2 + i) * 20 + (blk >> 1)) * 1024 + lane * 16 + (blk & 1) * 8;
      x1v[i][j] = *(const u32x2*)(ws + OFF_X1 + o);
      sav[i][j] = *(const u32x2*)(ws + OFF_SA + o);
    }
  consume<C, NBW, true, true>(h2, ex, fa, lds, a, 9, 12, ones);
  STAMP(3, 2); CSTAMP(3, 7);
  CBAR();
  LAS char* xs = lds + (wm * 32 + tk) * 160 + wn * 96 + g * 8;
#pragma unroll
  for (int i = 0; i < 2; i++)
#pragma unroll
    for (int j = 0; j < NBW; j++) {
      LAS u32x2* px = (LAS u32x2*)(xs + i * 16 * 160 + j * 32);
      const u32x2 xv = *px;
      u32x2 o;
#pragma unroll
      for (int e = 0; e < 2; e++) {
        f32x2 bb = unpk(x1v[i][j][e]) + (e ? hi2(h2[i][j]) : lo2(h2[i][j]));
        f32x2 z = bb * -1.4426950408889634f;
        f32x2 d = f32x2{ex2(z[0]), ex2(z[1])} + 1.0f;
        f32x2 sl = bb * f32x2{rcp(d[0]), rcp(d[1])};
        o[e] = pk2v(unpk(xv[e]) + unpk(sav[i][j][e]) * sl);
      }
      *px = o;
    }
  CBAR();
  const int t = (wm * 2 + wn) * 64 + lane;
#pragma unroll
  for (int i = 0; i < 3; i++) {
    const int ch = t + 256 * i;
    if (ch < 640) {
      const int row = ch / 10, c = ch % 10;
      *(u32x4*)(out + (size_t)(rt * 64 + row) * 1280 + ct * 160 + c * 16) = *(LAS const u32x4*)(lds + ch * 16);
    }
  }
  STAMPEND(3);
}
extern "C" __global__ __launch_bounds__(512) void k_phase4(const char* __restrict__ x, char* __restrict__ ws, char* __restrict__ out) {
  typedef CfgC C;
  const int b = blockIdx.x, xcd = b & 7, idx = b >> 3;
  const int rt = (xcd >> 1) * 8 + (idx & 7), ct = (xcd & 1) * 4 + (idx >> 3);
  const int wid = __builtin_amdgcn_readfirstlane(threadIdx.x >> 6), lane = threadIdx.x & 63;
  LAS char* lds = (LAS char*)smem;
  STAMP(3, 0);
  if (wid >= 4) {
    const int w = wid - 4;
    unsigned tv[C::PT], wv[C::PW];
    make_tokv<C>(tv, w, lane, 48); make_wv<C>(wv, w, lane, 40);
    const char* xb = x + (size_t)rt * 64 * 1280 + ct * 160;
    auto ext = [&](unsigned dst) {
#pragma unroll
      for (int i = 0; i < 3; i++) {
        const unsigned ch = min((i * 4 + w) * 64 + lane, 639);
        dma16(xb, (ch / 10) * 1280 + (ch % 10) * 16, dst + (i * 4 + w) * 1024);
      }
    };
    auto fin = [&]() { waitvm<0>(); BAR(); };
    const unsigned junk = (unsigned)(size_t)lds + LDS_AUX + 3584;
    auto pre = [&]() {
      if (L2T) {
        const unsigned t = w * 64 + lane;
#pragma unroll
        for (int i = 0; i < 2; i++) {
          const unsigned la = (idx >> 3) * 320 + min(t + i * 256, 319u);
          dma4(ws + OFF_H3 + (size_t)rt * 4 * 48 * 1024, (la / 320) * 49152 + 8192 + (la % 320) * 128, junk);
        }
        const unsigned lb = (idx & 7) * 200 + min(t, 199u);
        dma4(ws + OFF_W4 + ct * 5 * 1024, (8 + lb / 40) * 40960 + (lb % 40) * 128, junk);
      }
      touch_code(idx * 4 + w, lane, junk);
    };
    loader<C, XC + 3 * L2T, 3>(w, (unsigned)(size_t)lds, ws + OFF_H3 + (size_t)rt * 4 * 48 * 1024, 4096, tv, ws + OFF_W4 + ct * 5 * 1024, 4 * 40 * 1024, wv, 12, pre, Nop(), ext, fin);
    STAMP(3, 1);
    return;
  }
  const int wm = wid >> 1, wn = wid & 1;
  if (wn == 0) p4_consumer<3>(out, ws, lds, rt, ct, wm, wn, lane);
  else p4_consumer<2>(out, ws, lds, rt, ct, wm, wn, lane);
}

extern "C" void kernel_launch(void* const* d_in, const int* in_sizes, int n_in, void* d_out, int out_size, void* d_ws, size_t ws_size, hipStream_t stream) {
  const char* x = (const char*)d_in[0]; const char* wqkv = (const char*)d_in[1]; const char* wo = (const char*)d_in[2];
  const char* w1 = (const char*)d_in[4]; const char* w2 = (const char*)d_in[5]; const char* w3 = (const char*)d_in[6]; const char* w4 = (const char*)d_in[7];
#define WSOFF 0
  char* ws = (char*)d_ws;
  if (WSOFF == 1) ws += (ws_size - (32u << 20)) & ~(size_t)0x1fffff;
  if (WSOFF == 2) ws += (ws_size / 2) & ~(size_t)0x1fffff;
  (void)hipFuncSetAttribute((const void*)k_phase1, hipFuncAttributeMaxDynamicSharedMemorySize, LDS_TOTAL);
  (void)hipFuncSetAttribute((const void*)k_phase2, hipFuncAttributeMaxDynamicSharedMemorySize, LDS_TOTAL);
  (void)hipFuncSetAttribute((const void*)k_phase3, hipFuncAttributeMaxDynamicSharedMemorySize, LDS_TOTAL);
  (void)hipFuncSetAttribute((const void*)k_phase4, hipFuncAttributeMaxDynamicSharedMemorySize, LDS_TOTAL);
  k_phase1<<<256, 512, LDS_TOTAL, stream>>>(x, wqkv, w1, ws, wo, w2, w3, w4);
  k_phase2<<<256, 512, LDS_TOTAL, stream>>>(x, wqkv, wo, w2, w4, ws);
  k_phase3<<<256, 512, LDS_TOTAL, stream>>>(w3, ws);
  k_phase4<<<256, 512, LDS_TOTAL, stream>>>(x, ws, (char*)d_out);
}
```

```cpp
#include <hip/hip_runtime.h>
#include <stdint.h>

typedef __attribute__((ext_vector_type(8))) __bf16 bf16x8;
typedef __attribute__((ext_vector_type(2))) __bf16 bf16x2;
typedef __attribute__((ext_vector_type(4))) float f32x4;
typedef __attribute__((ext_vector_type(2))) float f32x2;
typedef __attribute__((ext_vector_type(4))) short v4i16;
typedef __attribute__((ext_vector_type(8))) short v8i16;
typedef __attribute__((ext_vector_type(4))) unsigned u32x4;
typedef __attribute__((ext_vector_type(2))) unsigned u32x2;
#define LAS __attribute__((address_space(3)))
#define DI __device__ __forceinline__
#define SB() __builtin_amdgcn_sched_barrier(0)

constexpr int S_ = 2048, D_ = 640, F_ = 1536;
constexpr size_t KB = 1024;
constexpr size_t OFF_PK = 0;
constexpr size_t OFF_V = OFF_PK + 64 * 40 * KB;
constexpr size_t OFF_H = OFF_V + 64 * 40 * KB;
constexpr size_t OFF_X1 = OFF_H + 128 * 20 * KB;
constexpr size_t OFF_H3 = OFF_X1 + 128 * 20 * KB;
constexpr size_t OFF_PQ = OFF_H3 + 128 * 48 * KB;
constexpr size_t OFF_SA = OFF_PQ + 128 * 20 * KB;
constexpr size_t OFF_W4 = OFF_SA + 128 * 24 * KB;
constexpr size_t OFF_ME = OFF_W4 + 48 * 40 * KB;
constexpr size_t OFF_ZP = OFF_ME + 20 * 40 * KB;
constexpr size_t OFF_ST = OFF_ZP + 16 * 640 * 4;
constexpr size_t OFF_XS = OFF_ST + 2048 * 8;
constexpr size_t OFF_CS = OFF_XS + 10 * 2048 * 8;
constexpr float EPS = 1e-5f;
#define STAMP(k, i)
#define STAMPEND(k)
#define CSTAMP(k, i)

DI void dma16(const void* sbase, unsigned voff, unsigned ldsdst) {
  unsigned keep;
  asm volatile("s_mov_b32 %0, m0\n\ts_mov_b32 m0, %3\n\ts_nop 0\n\tglobal_load_lds_dwordx4 %1, %2\n\ts_mov_b32 m0, %0"
               : "=&s"(keep) : "v"(voff), "s"(sbase), "s"(ldsdst) : "memory");
}
DI void dma4(const void* sbase, unsigned voff, unsigned ldsdst) {
  unsigned keep;
  asm volatile("s_mov_b32 %0, m0\n\ts_mov_b32 m0, %3\n\ts_nop 0\n\tglobal_load_lds_dword %1, %2\n\ts_mov_b32 m0, %0"
               : "=&s"(keep) : "v"(voff), "s"(sbase), "s"(ldsdst) : "memory");
}
#define CODE_ADDR(sym, out) asm volatile("s_getpc_b64 s[40:41]\n\ts_add_u32 s40, s40, " sym "@rel32@lo+4\n\ts_addc_u32 s41, s41, " sym "@rel32@hi+12\n\ts_mov_b64 %0, s[40:41]" \
                                         : "=s"(out) :: "s40", "s41", "scc")
#define T1 0
#define CSTR 64
#define L2T 0
constexpr unsigned P4LEN = 9088;
constexpr int XC = 1;
DI void touch_code(unsigned gid, unsigned lane, unsigned ldsdst) {
  constexpr int dummy = 0; (void)dummy;
  unsigned long long a, b;
  CODE_ADDR("k_phase1", a); CODE_ADDR("k_phase4", b);
  const unsigned len = min((unsigned)(b - a) + P4LEN, 131072u), o = (gid * 64 + lane) * CSTR;
  dma4((const char*)a, min(o, len - 128), ldsdst);
}
#define WT 15
#define WT8 15
#define T2 0
#define PFD 4
#define PF1 1
#define PT_ 0
#define SPT 1
#define CSV 0
#define CSP 3
#define PFS 1
#define HPT 0
#define ARGF 0
#define BRF 0
#define LDF 1
#define XPF 3
#define PRIO 0
#define PST 0
template <int PH> DI void st16(void* p, u32x4 v) {
  if (WT & PH) asm volatile("global_store_dwordx4 %0, %1, off sc1\n\ts_nop 1" ::"v"(p), "v"(v) : "memory");
  else *(u32x4*)p = v;
}
template <int PH> DI void st4(void* p, float v) {
  if (WT8 & PH) asm volatile("global_store_dword %0, %1, off sc1\n\ts_nop 1" ::"v"(p), "v"(v) : "memory");
  else *(float*)p = v;
}
template <int PH> DI void st8(void* p, u32x2 v) {
  if (WT8 & PH) asm volatile("global_store_dwordx2 %0, %1, off sc1\n\ts_nop 1" ::"v"(p), "v"(v) : "memory");
  else *(u32x2*)p = v;
}
DI float dot2(unsigned a, unsigned b, float c) { return __builtin_amdgcn_fdot2_f32_bf16(__builtin_bit_cast(bf16x2, a), __builtin_bit_cast(bf16x2, b), c, false); }
#define BAR() asm volatile("s_waitcnt lgkmcnt(0)\n\ts_barrier" ::: "memory")
template <int N> DI void waitvm() { asm volatile("s_waitcnt vmcnt(%0)" ::"n"(N) : "memory"); }
DI unsigned pk2(float lo, float hi) { f32x2 v = {lo, hi}; bf16x2 b = __builtin_convertvector(v, bf16x2); return __builtin_bit_cast(unsigned, b); }
DI unsigned pk2v(f32x2 v) { bf16x2 b = __builtin_convertvector(v, bf16x2); return __builtin_bit_cast(unsigned, b); }
DI float bflo(unsigned u) { return __uint_as_float(u << 16); }
DI float bfhi(unsigned u) { return __uint_as_float(u & 0xffff0000u); }
DI f32x2 unpk(unsigned u) { return f32x2{bflo(u), bfhi(u)}; }
DI u32x2 asu(f32x2 v) { return __builtin_bit_cast(u32x2, v); }
DI float ex2(float x) { return __builtin_amdgcn_exp2f(x); }
DI float rcp(float x) { return __builtin_amdgcn_rcpf(x); }
DI f32x2 lo2(f32x4 v) { return f32x2{v[0], v[1]}; }
DI f32x2 hi2(f32x4 v) { return f32x2{v[2], v[3]}; }
DI f32x2 gelu2(f32x2 t) {
  const float c1 = -1.5957691216057308f * 1.4426950408889634f, c2 = c1 * 0.044715f;
  f32x2 u = t * t;
  f32x2 z = t * (u * c2 + c1);
  f32x2 d = f32x2{ex2(z[0]), ex2(z[1])} + 1.0f;
  return t * f32x2{rcp(d[0]), rcp(d[1])};
}
DI f32x2 phi2(f32x2 t) {
  f32x2 a = t + (1.0f + 1e-6f), z = t * 1.4426950408889634f;
  f32x2 e = f32x2{ex2(z[0]), ex2(z[1])} + 1e-6f;
  return f32x2{t[0] > 0.f ? a[0] : e[0], t[1] > 0.f ? a[1] : e[1]};
}
template <int CTRL> DI float dppadd(float v) { return v + __builtin_bit_cast(float, __builtin_amdgcn_update_dpp(0, __builtin_bit_cast(int, v), CTRL, 0xf, 0xf, true)); }
DI float sum8(float v) { return dppadd<0x141>(dppadd<0x4E>(dppadd<0xB1>(v))); }
DI float sumrows(float v) {
  const auto a = __builtin_amdgcn_permlane16_swap(__builtin_bit_cast(unsigned, v), __builtin_bit_cast(unsigned, v), false, false);
  const float t = __builtin_bit_cast(float, (unsigned)a[0]) + __builtin_bit_cast(float, (unsigned)a[1]);
  const auto b = __builtin_amdgcn_permlane32_swap(__builtin_bit_cast(unsigned, t), __builtin_bit_cast(unsigned, t), false, false);
  return __builtin_bit_cast(float, (unsigned)b[0]) + __builtin_bit_cast(float, (unsigned)b[1]);
}
DI float shx(float v, int m) { return __shfl_xor(v, m, 64); }
DI bf16x8 lds128(LAS const char* p) { return *(LAS const bf16x8*)p; }
DI v4i16 ldstr(LAS const char* p) { return __builtin_amdgcn_ds_read_tr16_b64_v4i16((LAS v4i16*)p); }
DI bf16x8 cat(v4i16 a, v4i16 b) { v8i16 r = __builtin_shufflevector(a, b, 0, 1, 2, 3, 4, 5, 6, 7); return __builtin_bit_cast(bf16x8, r); }
DI f32x4 mfma(bf16x8 a, bf16x8 b, f32x4 c) { return __builtin_amdgcn_mfma_f32_16x16x32_bf16(a, b, c, 0, 0, 0); }

template <int MB_, int NB_, int NB0_, int KS_, int D_R, bool XROW_, bool FRAGW_>
struct Cfg {
  static constexpr int MB = MB_, NB = NB_, NB0 = NB0_, KS = KS_, D = D_R;
  static constexpr bool XROW = XROW_, FRAGW = FRAGW_;
  static constexpr int MBW = MB / 2, RB = NB * 32, TOKB = MB * KS * 1024, WB = KS * 32 * RB, STAGE = TOKB + WB;
  static constexpr int PT = TOKB / 4096, PW = WB / 4096, P = PT + PW;
};
typedef Cfg<8, 8, 4, 2, 4, true, false> CfgA1;
typedef Cfg<8, 8, 4, 2, 4, false, false> CfgA;
typedef Cfg<4, 10, 5, 2, 4, true, false> CfgQ;
typedef Cfg<8, 10, 5, 2, 4, false, true> CfgS;
typedef Cfg<4, 5, 3, 4, 4, false, true> CfgC;
constexpr int LDS_AUX = 147456;
constexpr int LDS_TOTAL = LDS_AUX + 8192;
#define RLX __ATOMIC_RELAXED, __HIP_MEMORY_SCOPE_AGENT

template <int RB> DI unsigned wswz(unsigned rho) { return RB == 256 ? 2 * (rho & 7) : (RB == 320 ? 2 * ((rho >> 2) & 1) : 0); }

template <class C, int XT, int XE, class Pre, class Post, class Ext, class Fin>
DI void loader(int w, unsigned lds0, const char* tokbase, int tokstride, const unsigned (&tokv)[C::PT],
               const char* wb, int wstride, const unsigned (&wv)[C::PW], int NS, Pre pre, Post post, Ext ext, Fin fin) {
  static_assert(C::D == 4, "ring depth");
  auto issue = [&](int s) {
    const char* tb = tokbase + (size_t)s * tokstride;
    const char* wp = wb + (size_t)s * wstride;
    unsigned dst = lds0 + (s & 3) * C::STAGE + w * 1024;
#pragma unroll
    for (int i = 0; i < C::PT; i++) dma16(tb, tokv[i], dst + i * 4096);
#pragma unroll
    for (int i = 0; i < C::PW; i++) dma16(wp, wv[i], dst + C::TOKB + i * 4096);
  };
  issue(0); issue(1); pre();
  waitvm<C::P + XT>(); BAR();
  issue(2); issue(3); post(0);
  waitvm<2 * C::P + XT>(); BAR();
  issue(4); post(1);
#pragma unroll 1
  for (int j = 2; j < NS - 3; j++) {
    waitvm<2 * C::P>(); BAR();
    issue(j + 3); post(j);
  }
  waitvm<2 * C::P>(); BAR();
  ext(lds0 + (NS & 3) * C::STAGE); post(NS - 3);
  waitvm<C::P + XE>(); BAR();
  post(NS - 2);
  waitvm<XE>(); BAR();
  post(NS - 1);
  fin();
}
struct Nop { DI void operator()() const {} DI void operator()(int) const {} DI void operator()(unsigned) const {} };

template <class C> DI void make_wv(unsigned (&wv)[C::PW], unsigned w, unsigned lane, unsigned pitch) {
#pragma unroll
  for (int i = 0; i < C::PW; i++) {
    if (C::FRAGW) {
      unsigned pc = i * 4 + w, ks = pc / C::NB, nb = pc % C::NB;
      wv[i] = (ks * pitch + nb) * 1024 + lane * 16;
    } else {
      unsigned sidx = (i * 4 + w) * 64 + lane;
      constexpr unsigned SL = C::RB / 16;
      unsigned rho = sidx / SL, c = sidx % SL;
      unsigned k = C::XROW ? ((rho & ~31u) | (((rho >> 2) & 3) << 3) | (((rho >> 4) & 1) << 2) | (rho & 3)) : rho;
      wv[i] = k * pitch + ((c ^ wswz<C::RB>(rho)) << 4);
    }
  }
}
template <class C> DI void make_tokv(unsigned (&tv)[C::PT], unsigned w, unsigned lane, unsigned kstot) {
#pragma unroll
  for (int i = 0; i < C::PT; i++) {
    unsigned qd = i * 4 + w;
    if (C::XROW) { unsigned row = qd * 8 + (lane >> 3); tv[i] = row * 1280 + (((lane & 7) ^ ((row >> 1) & 7)) << 4); }
    else { unsigned mb = qd / C::KS, ks = qd % C::KS; tv[i] = (mb * kstot + ks) * 1024 + lane * 16; }
  }
}

template <class C, int NBW> struct Frags { bf16x8 t[C::MBW]; v4i16 wl[NBW], wh[NBW]; bf16x8 wf[NBW]; };
template <class C> struct CAddr { int tok[2]; int tokb[2]; int wa[4]; };
template <class C> DI void make_caddr(CAddr<C>& a, int wm, int wn, int lane, int pi = 0, int pj = 0) {
  const int g = lane >> 4, q = (lane >> 2) & 3, p = lane & 3, t = lane & 15;
  if (C::XROW) {
    a.tok[0] = (wm * C::MBW * 16 + t) * 128 + ((g ^ ((t >> 1) & 7)) << 4);
    a.tok[1] = (wm * C::MBW * 16 + t) * 128 + (((4 + g) ^ ((t >> 1) & 7)) << 4);
  } else { a.tok[0] = wm * C::MBW * C::KS * 1024 + lane * 16; a.tok[1] = 0; }
  {
    const int rot = pi * (C::XROW ? 2048 : C::KS * 1024);
    a.tokb[0] = a.tok[0] - rot; a.tokb[1] = a.tok[1] - rot; a.tok[0] += rot; a.tok[1] += rot;
  }
  int r0 = 4 * g + q, base = C::TOKB + r0 * C::RB + (p >> 1) * 16 + (p & 1) * 8;
  if (C::FRAGW) { a.wa[0] = C::TOKB + wn * C::NB0 * 1024 + lane * 16; a.wa[1] = a.wa[2] = a.wa[3] = 0; }
  else if (C::RB == 256) {
#pragma unroll
    for (int j = 0; j < 4; j++) a.wa[j] = base + (((wn * 4 + ((j + pj) & 3)) ^ (r0 & 7)) << 5);
  } else {
    int b = g & 1, n0 = wn * C::NB0;
    a.wa[0] = base + ((n0 ^ b) << 5);
    a.wa[1] = base + (((n0 + 1) ^ b) << 5) - 32;
    a.wa[2] = a.wa[3] = 0;
  }
}
template <class C, int NBW> struct RD { static constexpr int WR = C::FRAGW ? 1 : 2, NR = C::MBW + WR * NBW; };
template <class C, int NBW>
DI void read_one(Frags<C, NBW>& f, LAS const char* slot, const CAddr<C>& a, int ks, int r) {
  constexpr int WR = RD<C, NBW>::WR;
  int ti = -1, wj = -1, h = 0;
  if (r == 0) ti = 0;
  else if (r <= WR) { wj = 0; h = r - 1; }
  else if (r < WR + C::MBW) ti = r - WR;
  else { int q = r - C::MBW; wj = q / WR; h = q % WR; }
  if (ti >= 0) {
    const bool hi = ti >= C::MBW / 2;
    f.t[ti] = C::XROW ? lds128(slot + (hi ? a.tokb[ks & 1] : a.tok[ks & 1]) + ti * 2048)
                      : lds128(slot + (hi ? a.tokb[0] : a.tok[0]) + (ti * C::KS + ks) * 1024);
  } else if (C::FRAGW) {
    f.wf[wj] = lds128(slot + a.wa[0] + (ks * C::NB + wj) * 1024);
  } else {
    LAS const char* pw = C::RB == 256 ? slot + a.wa[wj] : slot + a.wa[wj & 1] + wj * 32;
    if (h == 0) f.wl[wj] = ldstr(pw + ks * 32 * C::RB);
    else f.wh[wj] = ldstr(pw + (ks * 32 + 16) * C::RB);
  }
}
template <class C, int NBW> DI bf16x8 wfrag(const Frags<C, NBW>& f, int j) { return C::FRAGW ? f.wf[j] : cat(f.wl[j], f.wh[j]); }
template <class C, int NBW>
DI void load_all(Frags<C, NBW>& f, LAS const char* slot, const CAddr<C>& a, int ks) {
#pragma unroll
  for (int r = 0; r < RD<C, NBW>::NR; r++) read_one<C, NBW>(f, slot, a, ks, r);
}
template <int NCS_, int CSM_, int NTS_> struct Extra {
  static constexpr int NCS = NCS_, CSM = CSM_, NTS = NTS_;
  f32x4 cs[NCS_ ? NCS_ : 1], ts[NTS_ ? NTS_ : 1], tq[NTS_ ? NTS_ : 1];
};
template <class C, int NBW, bool SWAP, bool BARR, bool RDN, class E>
DI void kstep(f32x4 (&acc)[C::MBW][NBW], E& ex, const Frags<C, NBW>& cur, Frags<C, NBW>& nxt,
              LAS const char* nslot, const CAddr<C>& a, int nks, bf16x8 ones) {
  constexpr int MBW = C::MBW, NM = MBW * NBW, NR = RD<C, NBW>::NR;
  constexpr int F0 = BARR ? MBW : 0, NSL = NM - F0, PER = (NR + NSL - 1) / NSL;
#pragma unroll
  for (int m = 0; m < NM; m++) {
    const int j = m / MBW, i = m % MBW;
    bf16x8 wf = wfrag<C, NBW>(cur, j);
    acc[i][j] = SWAP ? mfma(wf, cur.t[i], acc[i][j]) : mfma(cur.t[i], wf, acc[i][j]);
    if (i == MBW - 1 && j < E::NCS) ex.cs[j] = E::CSM ? mfma(wf, ones, ex.cs[j]) : mfma(ones, wf, ex.cs[j]);
    if (j == 2 && i < E::NTS) ex.ts[i] = mfma(ones, cur.t[i], ex.ts[i]);
    if (j == 3 && i < E::NTS) ex.tq[i] = mfma(cur.t[i], cur.t[i], ex.tq[i]);
    if (BARR && m == F0 - 1) { SB(); BAR(); }
    if (RDN && m >= F0) {
#pragma unroll
      for (int r = (m - F0) * PER; r < (m - F0 + 1) * PER && r < NR; r++) read_one<C, NBW>(nxt, nslot, a, nks, r);
    }
    SB();
  }
}
template <class C, int NBW, bool SWAP, bool LAST, class E>
DI void consume(f32x4 (&acc)[C::MBW][NBW], E& ex, Frags<C, NBW>& fa, LAS const char* lds, const CAddr<C>& a, int s0, int s1, bf16x8 ones) {
  Frags<C, NBW> fb;
  if (PRIO) __builtin_amdgcn_s_setprio(PRIO);
  const int e = LAST ? s1 - 1 : s1;
#pragma unroll 1
  for (int s = s0; s < e; s++) {
    LAS const char* slot = lds + (s & 3) * C::STAGE;
    LAS const char* nsl = lds + ((s + 1) & 3) * C::STAGE;
#pragma unroll
    for (int ks = 0; ks < C::KS; ks += 2) {
      kstep<C, NBW, SWAP, false, true>(acc, ex, fa, fb, slot, a, ks + 1, ones);
      if (ks + 2 < C::KS) kstep<C, NBW, SWAP, false, true>(acc, ex, fb, fa, slot, a, ks + 2, ones);
      else kstep<C, NBW, SWAP, true, true>(acc, ex, fb, fa, nsl, a, 0, ones);
    }
  }
  if (LAST) {
    LAS const char* slot = lds + (e & 3) * C::STAGE;
#pragma unroll
    for (int ks = 0; ks < C::KS; ks += 2) {
      kstep<C, NBW, SWAP, false, true>(acc, ex, fa, fb, slot, a, ks + 1, ones);
      if (ks + 2 < C::KS) kstep<C, NBW, SWAP, false, true>(acc, ex, fb, fa, slot, a, ks + 2, ones);
      else kstep<C, NBW, SWAP, false, false>(acc, ex, fb, fa, slot, a, 0, ones);
    }
  }
}
template <int A, int B> DI void zero(f32x4 (&acc)[A][B]) {
#pragma unroll
  for (int i = 0; i < A; i++)
#pragma unroll
    for (int j = 0; j < B; j++) acc[i][j] = f32x4{0.f, 0.f, 0.f, 0.f};
}
template <int A, int M, int B> DI void zero(Extra<A, M, B>& e) {
#pragma unroll
  for (int i = 0; i < (A ? A : 1); i++) e.cs[i] = f32x4{0.f, 0.f, 0.f, 0.f};
#pragma unroll
  for (int i = 0; i < (B ? B : 1); i++) e.ts[i] = e.tq[i] = f32x4{0.f, 0.f, 0.f, 0.f};
}
DI bf16x8 ones_frag() { u32x4 o = {0x3f803f80u, 0x3f803f80u, 0x3f803f80u, 0x3f803f80u}; return __builtin_bit_cast(bf16x8, o); }
#define CBAR() asm volatile("s_waitcnt lgkmcnt(0)\n\ts_barrier" ::: "memory")

template <int NB, int NP = 2> DI void give(LAS char* lds, int w, int lane, const f32x4 (&a)[NB], int o) {
#pragma unroll
  for (int j = 0; j < NB; j++) *(LAS f32x4*)(lds + ((w * NP + o) * NB + j) * 1024 + lane * 16) = a[j];
}
template <int NB, int NP = 2> DI void take(LAS const char* lds, int w, int lane, f32x4 (&a)[NB], int o) {
#pragma unroll
  for (int j = 0; j < NB; j++) a[j] = *(LAS const f32x4*)(lds + ((w * NP + o) * NB + j) * 1024 + lane * 16);
}
template <int NB> DI void cpy(f32x4 (&d)[NB], const f32x4 (&a)[NB]) {
#pragma unroll
  for (int j = 0; j < NB; j++) d[j] = a[j];
}

extern __shared__ __attribute__((aligned(16))) char smem[];
#define AUXF ((LAS float*)(lds + LDS_AUX))
DI f32x4 ldsf4(LAS const float* p) { return *(LAS const f32x4*)p; }
DI float sel4(f32x4 v, int k) { float a = (k & 1) ? v[1] : v[0], b = (k & 1) ? v[3] : v[2]; return (k & 2) ? b : a; }

extern "C" __global__ __launch_bounds__(512) void k_phase1(const char* __restrict__ x, const char* __restrict__ wqkv, const char* __restrict__ w1, char* __restrict__ ws,
                                              const char* __restrict__ wo, const char* __restrict__ w2, const char* __restrict__ w3) {
  typedef CfgA1 C;
  const int b = blockIdx.x, xcd = b & 7, idx = b >> 3;
  const int rt = (xcd >> 1) * 4 + (idx & 3), ct = (xcd & 1) * 8 + (idx >> 2);
  const int wid = __builtin_amdgcn_readfirstlane(threadIdx.x >> 6), lane = threadIdx.x & 63;
  LAS char* lds = (LAS char*)smem;
  if (ARGF) asm volatile("" ::"s"(x), "s"(wqkv), "s"(w1), "s"(ws), "s"(wo), "s"(w2), "s"(w3));
  STAMP(0, 0);
  auto pf1 = [&]() {
    const unsigned junk = (unsigned)(size_t)lds + LDS_AUX + 7168, id = b * 4 + wid, c = id * 64 + lane;
    constexpr unsigned R0 = 64 * PFS, NR = 640 - R0, XT2 = NR * 160 - 65536;
    const unsigned xo = id * 2560 + 2 * R0 + min(lane, (int)(NR / 8 - 1)) * 16;
    dma16(x, xo, junk); dma16(x, xo + 1280, junk);
    dma16(wqkv, (R0 + c / 160) * 3840 + 1280 + (c % 160) * 16, junk);
    const unsigned c2 = 65536 + min(c, XT2 - 1);
    if (id < XT2 / 64) dma16(wqkv, (R0 + c2 / 160) * 3840 + 1280 + (c2 % 160) * 16, junk);
    if (id < NR * 80 / 64) dma16(w1, R0 * 1280 + c * 16, junk);
    if (PFS == 1 && id >= 768) dma16(x, ((c - 49152) >> 3) * 1280 + 1152 + (c & 7) * 16, junk);
  };
  auto pfu = [&](unsigned u, unsigned dst) {
    if (HPT) {
      if (u < 2560) { dma16(ws + OFF_H, u * 1024 + lane * 16, dst); return; }
      u = min(u - 2560, 2911u);
    }
    if (u < 800) dma16(w2, u * 1024 + lane * 16, dst);
    else if (u < 1600) dma16(wo, (u - 800) * 1024 + lane * 16, dst);
    else if (u < 2400) dma16(ws + OFF_ME, (u - 1600) * 1024 + lane * 16, dst);
    else dma16(ws + OFF_PQ, ((((u - 2400) >> 2) * 20 + (u & 3) * 5 + 2) * 64 + lane) * 16, dst);
  };
  const int hf = wid >> 2, wm = (wid >> 1) & 1, wn = wid & 1, g = lane >> 4, tk = lane & 15;
  f32x4 e[2][4];
  if (__builtin_expect(wid >= 4 && ct < 15, LDF)) {
    const int w = wid - 4;
    unsigned tv[C::PT], wv[C::PW];
    const char* wb = ct < 10 ? wqkv + (640 + ct * 128) * 2 : w1 + (ct - 10) * 256;
    const int pitch = ct < 10 ? 3840 : 1280;
    make_tokv<C>(tv, w, lane, 0); make_wv<C>(wv, w, lane, pitch);
    float s[4] = {0.f, 0.f, 0.f, 0.f}, q[4] = {0.f, 0.f, 0.f, 0.f};
    f32x2 s2[4] = {{0.f, 0.f}, {0.f, 0.f}, {0.f, 0.f}, {0.f, 0.f}}, q2[4] = {{0.f, 0.f}, {0.f, 0.f}, {0.f, 0.f}, {0.f, 0.f}};
    LAS const char* my = lds + w * 1024 + lane * 16;
    auto post = [&](int j) {
      LAS const char* p = my + (j & 3) * C::STAGE;
#pragma unroll
      for (int i = 0; i < 4; i++) {
        u32x4 v = *(LAS const u32x4*)(p + i * 4096);
#pragma unroll
        for (int e = 0; e < 4; e++) {
          if (PST) { const f32x2 u = unpk(v[e]); s2[i] += u; q2[i] = __builtin_elementwise_fma(u, u, q2[i]); }
          else { s[i] = dot2(v[e], 0x3f803f80u, s[i]); q[i] = dot2(v[e], v[e], q[i]); }
        }
      }
    };
    auto fin = [&]() {
#pragma unroll
      for (int i = 0; i < 4; i++) {
        if (PST) { s[i] = s2[i][0] + s2[i][1]; q[i] = q2[i][0] + q2[i][1]; }
        s[i] = sum8(s[i]); q[i] = sum8(q[i]);
      }
      const int i = lane & 3;
      const float sv = sel4(f32x4{s[0], s[1], s[2], s[3]}, i), qv = sel4(f32x4{q[0], q[1], q[2], q[3]}, i);
      const float m = sv * (1.0f / D_), var = fmaxf(qv * (1.0f / D_) - m * m, 0.f);
      const float rs = __builtin_amdgcn_rsqf(var + EPS);
      float r = rs;
      if (ct >= 10) r = rs * __builtin_amdgcn_rsqf(var * rs * rs + EPS);
      const int row = (i * 4 + w) * 8 + (lane >> 3);
      if ((lane & 7) < 4) {
        AUXF[row] = r; AUXF[128 + row] = r * m;
        if (ct == 0) st8<1>(ws + OFF_ST + (rt * 128 + row) * 8, asu(f32x2{m, rs}));
      }
      BAR();
    };
    const unsigned junk = (unsigned)(size_t)lds + LDS_AUX + 7168;
    auto pre = [&]() {
      if (T1) {
        const unsigned gid = min(b * 4 + w, 719);
        const char* p1 = gid < 320 ? x + gid * 8192 : (gid < 620 ? wqkv + (gid - 320) * 8192 : w1 + (gid - 620) * 8192);
        dma4(p1, lane * 128, junk);
      }
      touch_code(idx * 4 + w, lane, junk);
    };
    auto ext = [&](unsigned) {
#pragma unroll
      for (int i = 0; i < XPF; i++) pfu(i * 960 + (rt * 15 + ct) * 4 + w, junk);
    };
    loader<C, T1 + XC, XPF>(w, (unsigned)(size_t)lds, x + (size_t)rt * 128 * 1280, 128, tv, wb, 64 * pitch, wv, 10, pre, post, ext, fin);
    STAMP(0, 1);
    take<4>(lds, w, lane, e[0], 0); take<4>(lds, w, lane, e[1], 1);
  } else {
  if (PF1 == 1 && wid < 4) pf1();
  if (ct >= 15) {
    __builtin_amdgcn_s_sleep(PFD);
#pragma unroll 1
    for (int job = rt; job < 17; job += 16) {
      const char* wsrc = job < 5 ? wqkv + job * 256 : w3 + (job - 5) * 256;
      const unsigned pitch = job < 5 ? 3840 : 3072;
      const unsigned o0 = (wid * 80 + (lane >> 4)) * pitch + (lane & 15) * 16;
      u32x4 v[20];
#pragma unroll
      for (int i = 0; i < 20; i++) v[i] = *(const u32x4*)(wsrc + o0 + i * 4 * pitch);
      f32x2 c[4] = {{0.f, 0.f}, {0.f, 0.f}, {0.f, 0.f}, {0.f, 0.f}};
#pragma unroll
      for (int i = 0; i < 20; i++)
#pragma unroll
        for (int e = 0; e < 4; e++) c[e] += unpk(v[i][e]);
      LAS float* red = (LAS float*)(lds + 16384);
      *(LAS f32x4*)(red + (wid * 4 + (lane >> 4)) * 128 + (lane & 15) * 8) = f32x4{c[0][0], c[0][1], c[1][0], c[1][1]};
      *(LAS f32x4*)(red + (wid * 4 + (lane >> 4)) * 128 + (lane & 15) * 8 + 4) = f32x4{c[2][0], c[2][1], c[3][0], c[3][1]};
      BAR();
      if (wid < 2) {
        float t = 0.f;
#pragma unroll
        for (int r = 0; r < 32; r++) t += red[r * 128 + wid * 64 + lane];
        st4<1>(ws + OFF_CS + (job * 128 + wid * 64 + lane) * 4, t);
      }
      BAR();
    }
    const unsigned dstl = (unsigned)(size_t)lds + wid * 1024;
    if (rt > 0) {
#pragma unroll 1
      for (unsigned u = 960 * XPF + (rt - 1) * 8 + wid; u < 2912 + 2560 * HPT; u += 120) {
        pfu(u, dstl);
        waitvm<32>();
      }
    }
    STAMP(0, 4);
    waitvm<0>();
    STAMP(0, 5);
    return;
  }
  CAddr<C> a; make_caddr<C>(a, wm, wn, lane, 0, 2 * wm);
  f32x4 acc[4][4]; zero(acc);
  Extra<2, 0, 0> ex; zero(ex);
  const bf16x8 ones = ones_frag();
  Frags<C, 4> fa;
  CBAR(); STAMP(0, 1); CSTAMP(0, 6); load_all<C, 4>(fa, lds, a, 0);
  if (ct < 10) consume<C, 4, false, true>(acc, ex, fa, lds, a, 0, 10, ones);
  else consume<C, 4, true, true>(acc, ex, fa, lds, a, 0, 10, ones);
  STAMP(0, 2); CSTAMP(0, 7);
  if (g == 0) {
#pragma unroll
    for (int i = 0; i < 2; i++) AUXF[256 + wn * 64 + (2 * wm + i) * 16 + tk] = ex.cs[i][0];
  }
  give<4>(lds, wid, lane, acc[2], 0); give<4>(lds, wid, lane, acc[3], 1);
  cpy<4>(e[0], acc[0]); cpy<4>(e[1], acc[1]);
  CBAR();
  }
  STAMP(0, 3);
  if (ct < 10) {
    char* dst = ws + (ct < 5 ? OFF_PK : OFF_V) + lane * 16;
    const int cb0 = (ct < 5 ? ct : ct - 5) * 8 + wn * 4;
    const int tb = wm * 64 + hf * 32 + 4 * g;
    const f32x4 r0 = ldsf4(AUXF + tb), r1 = ldsf4(AUXF + tb + 16), m0 = ldsf4(AUXF + 128 + tb), m1 = ldsf4(AUXF + 128 + tb + 16);
    char* d2 = dst + (size_t)((rt * 4 + wm * 2 + hf) * 40 + cb0) * 1024;
    float z[4];
#pragma unroll
    for (int j = 0; j < 4; j++) {
      const float cs = AUXF[256 + wn * 64 + ((j + 2 * wm) & 3) * 16 + tk];
      f32x4 t0 = r0 * e[0][j] - m0 * cs, t1 = r1 * e[1][j] - m1 * cs;
      u32x4 o;
      if (ct < 5) {
        o = u32x4{pk2v(phi2(lo2(t0))), pk2v(phi2(hi2(t0))), pk2v(phi2(lo2(t1))), pk2v(phi2(hi2(t1)))};
        f32x2 zs = (unpk(o[0]) + unpk(o[1])) + (unpk(o[2]) + unpk(o[3]));
        z[j] = zs[0] + zs[1];
      } else {
        o = u32x4{pk2v(lo2(t0)), pk2v(hi2(t0)), pk2v(lo2(t1)), pk2v(hi2(t1))};
      }
      st16<1>(d2 + ((j + 2 * wm) & 3) * 1024, o);
    }
    if (ct < 5) {
#pragma unroll
      for (int j = 0; j < 4; j++) z[j] = sumrows(z[j]);
      if (lane < 16) {
#pragma unroll
        for (int j = 0; j < 4; j++) AUXF[512 + (wm * 2 + hf) * 128 + wn * 64 + ((j + 2 * wm) & 3) * 16 + lane] = z[j];
      }
      CBAR();
      if (wid < 2) {
        const int t = wid * 64 + lane;
        st4<1>(ws + OFF_ZP + (rt * 640 + ct * 128 + t) * 4, (AUXF[512 + t] + AUXF[640 + t]) + (AUXF[768 + t] + AUXF[896 + t]));
      }
    }
  } else {
    const int c0 = (ct - 10) * 128 + wn * 64;
#pragma unroll
    for (int i = 0; i < 2; i++) {
      const int row = wm * 64 + hf * 32 + i * 16 + tk;
      const float rr = AUXF[row], rm = AUXF[128 + row];
#pragma unroll
      for (int jp = 0; jp < 2; jp++) {
        const int jt = jp ^ wm;
        u32x4 o;
#pragma unroll
        for (int h = 0; h < 2; h++) {
          f32x4 cs = ldsf4(AUXF + 256 + wn * 64 + (2 * jt + h) * 16 + 4 * g);
          f32x4 t = rr * e[i][2 * jp + h] - rm * cs;
          o[2 * h] = pk2v(gelu2(lo2(t))); o[2 * h + 1] = pk2v(gelu2(hi2(t)));
        }
        const int col = c0 + jt * 32, grp = col / 160;
        const int dl = grp == 0 ? 1 : (grp == 1 ? -1 : (grp == 2 ? 2 : -2));
        int td = rt * 128 + row + dl;
        const bool ok = td >= 0 && td < S_;
        td &= S_ - 1;
        if (!ok) o = u32x4{0, 0, 0, 0};
        st16<1>(ws + OFF_H + ((size_t)((td >> 4) * 20 + (col >> 5)) * 64 + g * 16 + (td & 15)) * 16, o);
      }
    }
  }
  waitvm<0>();
  STAMPEND(0);
}

extern "C" __global__ __launch_bounds__(512) void k_phase2(const char* __restrict__ x, const char* __restrict__ wqkv, const char* __restrict__ wo, const char* __restrict__ w2,
                                              const char* __restrict__ w4, char* __restrict__ ws) {
  const int b = blockIdx.x;
  const int wid = __builtin_amdgcn_readfirstlane(threadIdx.x >> 6), lane = threadIdx.x & 63;
  LAS char* lds = (LAS char*)smem;
  const int g = lane >> 4, tk = lane & 15;
  if (ARGF) asm volatile("" ::"s"(x), "s"(wqkv), "s"(wo), "s"(w2), "s"(w4), "s"(ws));
  STAMP(1, 0);
  if (__builtin_expect(b < 128, BRF)) {
    typedef CfgQ C;
    const int rt = b >> 2, ct = b & 3;
    const int hf = wid >> 2, wm = (wid >> 1) & 1, wn = wid & 1;
    const f32x2 st = ((const f32x2*)(ws + OFF_ST))[rt * 64 + wm * 32 + hf * 16 + tk];
    f32x4 e[5];
    if (__builtin_expect(wid >= 4, LDF)) {
      const int w = wid - 4;
      unsigned tv[C::PT], wv[C::PW];
      make_tokv<C>(tv, w, lane, 0); make_wv<C>(wv, w, lane, 3840);
      loader<C, XC, 0>(w, (unsigned)(size_t)lds, x + (size_t)rt * 64 * 1280, 128, tv, wqkv + ct * 320, 64 * 3840, wv, 10, [&]() { touch_code((b >> 3) * 4 + w, lane, (unsigned)(size_t)lds + LDS_AUX + 7168); }, Nop(), Nop(), Nop());
      STAMP(1, 1);
      BAR();
      take<5, 1>(lds, w, lane, e, 0);
    } else {
      float zp[16], csv = 0.f;
      const int t = threadIdx.x;
      if (t < 160) {
        csv = ((const float*)(ws + OFF_CS))[ct * 160 + t];
        const float* p = (const float*)(ws + OFF_ZP) + ct * 160 + t;
#pragma unroll
        for (int r = 0; r < 16; r++) zp[r] = p[r * 640];
      }
      CAddr<C> a; make_caddr<C>(a, wm, wn, lane);
      f32x4 acc[2][5]; zero(acc);
      Extra<0, 0, 0> ex;
      const bf16x8 ones = ones_frag();
      Frags<C, 5> fa;
      CBAR(); STAMP(1, 1); CSTAMP(1, 6); load_all<C, 5>(fa, lds, a, 0);
      if (t < 160) {
        float z = 0.f;
#pragma unroll
        for (int r = 0; r < 16; r += 4) z += (zp[r] + zp[r + 1]) + (zp[r + 2] + zp[r + 3]);
        AUXF[512 + t] = z; AUXF[256 + t] = csv;
      }
      consume<C, 5, true, true>(acc, ex, fa, lds, a, 0, 10, ones);
      STAMP(1, 2); CSTAMP(1, 7);
      give<5, 1>(lds, wid, lane, acc[1], 0);
      cpy<5>(e, acc[0]);
      CBAR();
    }
    STAMP(1, 3);
    {
      const float rs = st[1], rm = rs * st[0];
      f32x4 pq[5]; float part[5];
#pragma unroll
      for (int j = 0; j < 5; j++) {
        const f32x4 z = ldsf4(AUXF + 512 + wn * 80 + j * 16 + 4 * g), cs = ldsf4(AUXF + 256 + wn * 80 + j * 16 + 4 * g);
        f32x4 tt = rs * e[j] - rm * cs;
        f32x2 p0 = phi2(lo2(tt)), p1 = phi2(hi2(tt));
        pq[j] = f32x4{p0[0], p0[1], p1[0], p1[1]};
        f32x2 s = p0 * lo2(z) + p1 * hi2(z);
        part[j] = s[0] + s[1];
      }
      float dh[4];
      dh[0] = part[0] + (g == 0 ? part[1] : 0.f);
      dh[1] = (g >= 1 ? part[1] : 0.f) + (g <= 1 ? part[2] : 0.f);
      dh[2] = (g >= 2 ? part[2] : 0.f) + (g <= 2 ? part[3] : 0.f);
      dh[3] = (g == 3 ? part[3] : 0.f) + part[4];
#pragma unroll
      for (int h = 0; h < 4; h++) dh[h] = rcp(sumrows(dh[h]) + 1e-6f);
      const float sc[5] = {dh[0], g == 0 ? dh[0] : dh[1], g <= 1 ? dh[1] : dh[2], g <= 2 ? dh[2] : dh[3], dh[3]};
#pragma unroll
      for (int j = 0; j < 5; j++) pq[j] *= sc[j];
      char* dst = ws + OFF_PQ + ((size_t)(rt * 4 + wm * 2 + hf) * 20 + ct * 5) * 1024 + lane * 16;
      st16<2>(dst + (3 * wn) * 1024, u32x4{pk2v(lo2(pq[0])), pk2v(hi2(pq[0])), pk2v(lo2(pq[1])), pk2v(hi2(pq[1]))});
      st16<2>(dst + (3 * wn + 1) * 1024, u32x4{pk2v(lo2(pq[2])), pk2v(hi2(pq[2])), pk2v(lo2(pq[3])), pk2v(hi2(pq[3]))});
      st8<2>(dst + 2 * 1024 + wn * 8, u32x2{pk2v(lo2(pq[4])), pk2v(hi2(pq[4]))});
    }
    waitvm<0>();
    STAMPEND(1);
  } else if (__builtin_expect(b < 208, BRF)) {
    typedef CfgA C;
    const int bb = b - 128, rt = (bb & 7) * 2 + (bb >> 3) / 5, ct = (bb >> 3) % 5;
    const int hf = wid >> 2, wm = (wid >> 1) & 1, wn = wid & 1;
    f32x2 st[2];
#pragma unroll
    for (int i = 0; i < 2; i++) st[i] = ((const f32x2*)(ws + OFF_ST))[rt * 128 + wm * 64 + hf * 32 + i * 16 + tk];
    f32x4 e[2][4];
    if (__builtin_expect(wid >= 4, LDF)) {
      const int w = wid - 4;
      unsigned tv[C::PT], wv[C::PW];
      make_tokv<C>(tv, w, lane, 20); make_wv<C>(wv, w, lane, 1280);
      const char* xb = x + (size_t)rt * 128 * 1280 + ct * 256;
      const unsigned xr = w * 4 + g, xo = xr * 1280 + ((tk ^ xr) << 4);
      auto ext = [&](unsigned dst) {
#pragma unroll
        for (int i = 0; i < 8; i++) dma16(xb, xo + i * 16 * 1280, dst + (i * 4 + w) * 1024);
      };
      auto fin = [&]() { waitvm<0>(); BAR(); };
      loader<C, 0, 8>(w, (unsigned)(size_t)lds, ws + OFF_H + (size_t)rt * 8 * 20 * 1024, 2048, tv, w2 + ct * 256, 64 * 1280, wv, 10, Nop(), Nop(), ext, fin);
      STAMP(1, 1);
      take<4>(lds, w, lane, e[0], 0); take<4>(lds, w, lane, e[1], 1);
    } else {
      CAddr<C> a; make_caddr<C>(a, wm, wn, lane);
      f32x4 acc[4][4]; zero(acc);
      Extra<0, 0, 0> ex;
      const bf16x8 ones = ones_frag();
      Frags<C, 4> fa;
      CBAR(); STAMP(1, 1); CSTAMP(1, 6); load_all<C, 4>(fa, lds, a, 0);
      consume<C, 4, true, true>(acc, ex, fa, lds, a, 0, 10, ones);
      STAMP(1, 2); CSTAMP(1, 7);
      give<4>(lds, wid, lane, acc[2], 0); give<4>(lds, wid, lane, acc[3], 1);
      cpy<4>(e[0], acc[0]); cpy<4>(e[1], acc[1]);
      CBAR();
    }
    STAMP(1, 3);
    LAS const char* xs = lds + 2 * C::STAGE + (wm * 64 + hf * 32 + tk) * 256 + (g & 1) * 8;
#pragma unroll
    for (int i = 0; i < 2; i++) {
      const float rs = st[i][1], rm = st[i][0] * rs;
      f32x2 s1 = {0.f, 0.f}, s2 = {0.f, 0.f};
      char* dst = ws + OFF_X1 + ((size_t)(rt * 8 + wm * 4 + hf * 2 + i) * 20 + ct * 4 + wn * 2) * 1024 + lane * 16;
#pragma unroll
      for (int jp = 0; jp < 2; jp++) {
        u32x4 o;
#pragma unroll
        for (int h = 0; h < 2; h++) {
          u32x2 xx = *(LAS const u32x2*)(xs + i * 16 * 256 + (((wn * 8 + (2 * jp + h) * 2 + (g >> 1)) ^ tk) << 4));
          f32x4 c = e[i][2 * jp + h] - rm;
          o[2 * h] = pk2v(unpk(xx[0]) * rs + lo2(c));
          o[2 * h + 1] = pk2v(unpk(xx[1]) * rs + hi2(c));
        }
#pragma unroll
        for (int k = 0; k < 4; k++) { f32x2 v = unpk(o[k]); s1 += v; s2 += v * v; }
        st16<2>(dst + jp * 1024, o);
      }
      float a1 = s1[0] + s1[1], a2 = s2[0] + s2[1];
      a1 = sumrows(a1); a2 = sumrows(a2);
      if (g == 0) st8<2>(ws + OFF_XS + ((ct * 2 + wn) * 2048 + rt * 128 + wm * 64 + hf * 32 + i * 16 + tk) * 8, asu(f32x2{a1, a2}));
    }
    STAMPEND(1);
  } else if (b < 240) {
    const int h = b - 208, hh = h & 3, cb0 = 5 * (h >> 2) + hh;
    LAS char* wt = lds + 40960;
    {
      const char* wb = wo + (size_t)cb0 * 16 * 1280;
#pragma unroll
      for (int i = 0; i < 5; i++) {
        const unsigned pc = i * 8 + wid, s = pc * 64 + lane, qt = s / 640, r = (s % 640) / 20, c = s % 20;
        dma16(wb, r * 1280 + qt * 320 + ((c ^ (2 * ((r >> 2) & 1))) << 4), (unsigned)(size_t)wt + pc * 1024);
      }
    }
    bf16x8 pk[8][2], vv[8][2];
#pragma unroll
    for (int u = 0; u < 8; u++)
#pragma unroll
      for (int j = 0; j < 2; j++) {
        const size_t o = ((size_t)(wid * 8 + u) * 40 + cb0 + j) * 1024 + lane * 16;
        pk[u][j] = *(const bf16x8*)(ws + OFF_PK + o); vv[u][j] = *(const bf16x8*)(ws + OFF_V + o);
      }
    f32x4 acc[4];
#pragma unroll
    for (int i = 0; i < 4; i++) acc[i] = f32x4{0.f, 0.f, 0.f, 0.f};
#pragma unroll
    for (int u = 0; u < 8; u++)
#pragma unroll
      for (int i = 0; i < 4; i++) acc[i] = mfma(vv[u][i & 1], pk[u][i >> 1], acc[i]);
    STAMP(1, 2);
#pragma unroll
    for (int i = 0; i < 4; i++) *(LAS f32x4*)(lds + (wid * 4 + i) * 1024 + lane * 16) = acc[i];
    waitvm<0>();
    BAR();
    if (wid < 4) {
      f32x4 s = *(LAS const f32x4*)(lds + wid * 1024 + lane * 16);
#pragma unroll
      for (int w = 1; w < 8; w++) s += *(LAS const f32x4*)(lds + (w * 4 + wid) * 1024 + lane * 16);
      const unsigned ee = 4 * (wid & 1) + g - hh;
      u32x2 o = u32x2{pk2v(lo2(s)), pk2v(hi2(s))};
      if (ee >= 5u) o = u32x2{0u, 0u};
      *(LAS u32x2*)(lds + 32768 + wid * 512 + lane * 8) = o;
    }
    BAR();
    STAMP(1, 3);
    {
      const int q = (lane >> 2) & 3, p = lane & 3;
      bf16x8 af[2]; char* dst[2]; bool ok[2];
#pragma unroll
      for (int db = 0; db < 2; db++) {
        const u32x2 h0 = *(LAS const u32x2*)(lds + 32768 + (db * 2) * 512 + lane * 8), h1 = *(LAS const u32x2*)(lds + 32768 + (db * 2 + 1) * 512 + lane * 8);
        af[db] = __builtin_bit_cast(bf16x8, u32x4{h0[0], h0[1], h1[0], h1[1]});
        const int beta = cb0 + db, GG = beta / 10, j = beta % 10, wn = j / 5, jj = j % 5;
        const int kap = jj == 4 ? 5 * GG + 2 : 5 * GG + 3 * wn + (jj >> 1), tt = jj == 4 ? wn : (jj & 1);
        dst[db] = ws + OFF_ME + (size_t)kap * 40 * 1024 + lane * 16 + tt * 8;
        ok[db] = (unsigned)(4 * db + g - hh) < 5u;
      }
#pragma unroll
      for (int i = 0; i < 5; i++) {
        const int nb = wid * 5 + i, qt = nb / 10, nl = nb % 10;
        LAS const char* src = wt + qt * 10240 + (4 * g + q) * 320 + ((nl ^ (g & 1)) << 5) + p * 8;
        const bf16x8 wf = cat(ldstr(src), ldstr(src + 16 * 320));
#pragma unroll
        for (int db = 0; db < 2; db++) {
          const f32x4 c = mfma(af[db], wf, f32x4{0.f, 0.f, 0.f, 0.f});
          if (ok[db]) st8<2>(dst[db] + nb * 1024, u32x2{pk2v(lo2(c)), pk2v(hi2(c))});
        }
      }
    }
    STAMPEND(1);
  } else {
    LAS char* base = lds + wid * 16384;
    const unsigned lb = (unsigned)(size_t)base;
    const int q = (lane >> 2) & 3, p = lane & 3, r0 = 4 * g + q;
    const int u = (b - 240) * 8 + wid;
    if (u < 120) {
      const int kb = u / 5, cb = u % 5;
      const char* src = w4 + kb * 64 * 1280 + cb * 256;
      char* dst = ws + OFF_W4 + (kb * 80 + cb * 8) * 1024 + lane * 16;
#pragma unroll
      for (int i = 0; i < 16; i++) {
        const unsigned rho = i * 4 + g;
        dma16(src, rho * 1280 + (((lane & 15) ^ wswz<256>(rho)) << 4), lb + i * 1024);
      }
      waitvm<0>();
      LAS const char* pw = base + r0 * 256 + p * 8;
#pragma unroll
      for (int nb = 0; nb < 8; nb++)
#pragma unroll
        for (int ks = 0; ks < 2; ks++) {
          LAS const char* pp = pw + ((nb ^ (r0 & 7)) << 5) + ks * 32 * 256;
          st16<2>(dst + (ks * 40 + nb) * 1024, __builtin_bit_cast(u32x4, cat(ldstr(pp), ldstr(pp + 16 * 256))));
        }
    }
    STAMPEND(1);
  }
}

extern "C" __global__ __launch_bounds__(512) void k_phase3(const char* __restrict__ w3, char* __restrict__ ws) {
  const int b = blockIdx.x, xcd = b & 7;
  const int wid = __builtin_amdgcn_readfirstlane(threadIdx.x >> 6), lane = threadIdx.x & 63;
  LAS char* lds = (LAS char*)smem;
  const int wm = (wid >> 1) & 1, wn = wid & 1, g = lane >> 4, tk = lane & 15;
  if (ARGF) asm volatile("" ::"s"(w3), "s"(ws));
  STAMP(2, 0);
  if (__builtin_expect(b < 192, BRF)) {
    typedef CfgA C;
    const int idx = b >> 3;
    const int rt = (xcd >> 1) * 4 + (idx & 3), ct = (xcd & 1) * 6 + (idx >> 2);
    const int hf = wid >> 2;
    f32x4 e[2][4];
    if (__builtin_expect(wid >= 4, LDF)) {
      const int w = wid - 4;
      unsigned tv[C::PT], wv[C::PW];
      make_tokv<C>(tv, w, lane, 20); make_wv<C>(wv, w, lane, 3072);
      const unsigned junk = (unsigned)(size_t)lds + LDS_AUX + 7168;
      auto pre = [&]() {
        if (L2T) {
          const unsigned t = w * 64 + lane, la = min((idx >> 2) * 171 + min(t, 170u), 1023u), lb = (idx & 3) * 256 + t;
          dma4(ws + OFF_X1 + (size_t)rt * 8 * 20 * 1024, (la >> 7) * 20480 + 4096 + (la & 127) * 128, junk);
          dma4(w3 + ct * 256, (128 + (lb >> 1)) * 3072 + (lb & 1) * 128, junk);
        }
        touch_code(idx * 4 + w, lane, junk);
      };
      loader<C, XC + 2 * L2T, 0>(w, (unsigned)(size_t)lds, ws + OFF_X1 + (size_t)rt * 8 * 20 * 1024, 2048, tv, w3 + ct * 256, 64 * 3072, wv, 10, pre, Nop(), Nop(), Nop());
      STAMP(2, 1);
      BAR();
      take<4>(lds, w, lane, e[0], 0); take<4>(lds, w, lane, e[1], 1);
    } else {
    f32x2 xs[10]; float csv = 0.f;
    if (wid < 2) {
      csv = ((const float*)(ws + OFF_CS))[640 + ct * 128 + threadIdx.x];
      const f32x2* p = (const f32x2*)(ws + OFF_XS) + rt * 128 + threadIdx.x;
#pragma unroll
      for (int i = 0; i < 10; i++) xs[i] = p[i * 2048];
    }
    CAddr<C> a; make_caddr<C>(a, wm, wn, lane);
    f32x4 acc[4][4]; zero(acc);
    Extra<0, 0, 0> ex;
    const bf16x8 ones = ones_frag();
    Frags<C, 4> fa;
    CBAR(); STAMP(2, 1); CSTAMP(2, 6); load_all<C, 4>(fa, lds, a, 0);
    if (wid < 2) {
      f32x2 s = ((xs[0] + xs[1]) + (xs[2] + xs[3])) + ((xs[4] + xs[5]) + (xs[6] + xs[7])) + (xs[8] + xs[9]);
      float m = s[0] * (1.0f / D_), var = fmaxf(s[1] * (1.0f / D_) - m * m, 0.f);
      float rs = __builtin_amdgcn_rsqf(var + EPS);
      AUXF[threadIdx.x] = rs; AUXF[128 + threadIdx.x] = rs * m; AUXF[256 + threadIdx.x] = csv;
    }
    consume<C, 4, true, true>(acc, ex, fa, lds, a, 0, 10, ones);
    STAMP(2, 2); CSTAMP(2, 7);
    give<4>(lds, wid, lane, acc[2], 0); give<4>(lds, wid, lane, acc[3], 1);
    cpy<4>(e[0], acc[0]); cpy<4>(e[1], acc[1]);
    CBAR();
    }
    STAMP(2, 3);
#pragma unroll
    for (int i = 0; i < 2; i++) {
      const int row = wm * 64 + hf * 32 + i * 16 + tk;
      const float rs = AUXF[row], rm = AUXF[128 + row];
      char* dst = ws + OFF_H3 + ((size_t)(rt * 8 + wm * 4 + hf * 2 + i) * 48 + ct * 4 + wn * 2) * 1024 + lane * 16;
#pragma unroll
      for (int jp = 0; jp < 2; jp++) {
        u32x4 o;
#pragma unroll
        for (int h = 0; h < 2; h++) {
          const f32x4 cs = ldsf4(AUXF + 256 + wn * 64 + (2 * jp + h) * 16 + 4 * g);
          f32x4 t = rs * e[i][2 * jp + h] - rm * cs;
          o[2 * h] = pk2v(gelu2(lo2(t))); o[2 * h + 1] = pk2v(gelu2(hi2(t)));
        }
        st16<4>(dst + jp * 1024, o);
      }
    }
    waitvm<0>();
    STAMPEND(2);
  } else {
    typedef CfgS C;
    const int idx = (b - 192) >> 3;
    const int rt = (xcd >> 1) * 4 + (idx & 3), ct = (xcd & 1) * 2 + (idx >> 2);
    if (__builtin_expect(wid >= 4, LDF)) {
      const int w = wid - 4;
      unsigned tv[C::PT], wv[C::PW];
      make_tokv<C>(tv, w, lane, 20); make_wv<C>(wv, w, lane, 40);
      loader<C, 0, 0>(w, (unsigned)(size_t)lds, ws + OFF_PQ + (size_t)rt * 8 * 20 * 1024, 2048, tv, ws + OFF_ME + ct * 10 * 1024, 80 * 1024, wv, 10, Nop(), Nop(), Nop(), Nop());
      STAMP(2, 1);
      return;
    }
    CAddr<C> a; make_caddr<C>(a, wm, wn, lane);
    f32x4 acc[4][5]; zero(acc);
    Extra<0, 0, 0> ex;
    const bf16x8 ones = ones_frag();
    Frags<C, 5> fa;
    CBAR(); STAMP(2, 1); CSTAMP(2, 6); load_all<C, 5>(fa, lds, a, 0);
    consume<C, 5, true, true>(acc, ex, fa, lds, a, 0, 10, ones);
    STAMP(2, 2); CSTAMP(2, 7);
#pragma unroll
    for (int i = 0; i < 4; i++) {
      char* dst = ws + OFF_SA + ((size_t)(rt * 8 + wm * 4 + i) * 8 + ct * 2 + wn) * 3072 + lane * 16;
      st16<4>(dst, u32x4{pk2v(lo2(acc[i][0])), pk2v(hi2(acc[i][0])), pk2v(lo2(acc[i][1])), pk2v(hi2(acc[i][1]))});
      st16<4>(dst + 1024, u32x4{pk2v(lo2(acc[i][2])), pk2v(hi2(acc[i][2])), pk2v(lo2(acc[i][3])), pk2v(hi2(acc[i][3]))});
      if (i & 1) st16<4>(dst + 2048 - 8 * 3072, u32x4{pk2v(lo2(acc[i - 1][4])), pk2v(hi2(acc[i - 1][4])), pk2v(lo2(acc[i][4])), pk2v(hi2(acc[i][4]))});
    }
    waitvm<0>();
    STAMPEND(2);
  }
}

template <int NBW>
DI void p4_consumer(char* __restrict__ out, char* __restrict__ ws, LAS char* lds, int rt, int ct, int wm, int lane) {
  constexpr int wn = NBW == 3 ? 0 : 1;
  typedef CfgC C;
  const int g = lane >> 4, tk = lane & 15;
  u32x2 sav[2][NBW], x1v[2][NBW];
  CAddr<C> a; make_caddr<C>(a, wm, wn, lane);
  f32x4 h2[2][NBW]; zero(h2);
  Extra<0, 0, 0> ex;
  const bf16x8 ones = ones_frag();
  Frags<C, NBW> fa;
  CBAR(); STAMP(3, 1); CSTAMP(3, 6); load_all<C, NBW>(fa, lds, a, 0);
  consume<C, NBW, true, false>(h2, ex, fa, lds, a, 0, 9, ones);
#pragma unroll
  for (int i = 0; i < 2; i++)
#pragma unroll
    for (int j = 0; j < NBW; j++) {
      const int blk = ct * 5 + wn * 3 + j;
      const size_t o = ((size_t)(rt * 4 + wm * 2 + i) * 20 + (blk >> 1)) * 1024 + lane * 16 + (blk & 1) * 8;
      x1v[i][j] = *(const u32x2*)(ws + OFF_X1 + o);
      const int bl = wn * 3 + j;
      sav[i][j] = *(const u32x2*)(ws + OFF_SA + ((size_t)(rt * 4 + wm * 2 + (bl == 4 ? 0 : i)) * 8 + ct) * 3072 + (bl >> 1) * 1024 + lane * 16 + (bl == 4 ? i : (bl & 1)) * 8);
    }
  consume<C, NBW, true, true>(h2, ex, fa, lds, a, 9, 12, ones);
  STAMP(3, 2); CSTAMP(3, 7);
  CBAR();
  LAS char* xs = lds + (wm * 32 + tk) * 160 + wn * 96 + g * 8;
#pragma unroll
  for (int i = 0; i < 2; i++)
#pragma unroll
    for (int j = 0; j < NBW; j++) {
      LAS u32x2* px = (LAS u32x2*)(xs + i * 16 * 160 + j * 32);
      const u32x2 xv = *px;
      u32x2 o;
#pragma unroll
      for (int e = 0; e < 2; e++) {
        f32x2 bb = unpk(x1v[i][j][e]) + (e ? hi2(h2[i][j]) : lo2(h2[i][j]));
        f32x2 z = bb * -1.4426950408889634f;
        f32x2 d = f32x2{ex2(z[0]), ex2(z[1])} + 1.0f;
        f32x2 sl = bb * f32x2{rcp(d[0]), rcp(d[1])};
        o[e] = pk2v(unpk(xv[e]) + unpk(sav[i][j][e]) * sl);
      }
      *px = o;
    }
  CBAR();
  const int t = (wm * 2 + wn) * 64 + lane;
#pragma unroll
  for (int i = 0; i < 3; i++) {
    const int ch = t + 256 * i;
    if (ch < 640) {
      const int row = ch / 10, c = ch % 10;
      st16<8>(out + (size_t)(rt * 64 + row) * 1280 + ct * 160 + c * 16, *(LAS const u32x4*)(lds + ch * 16));
    }
  }
  waitvm<0>();
  STAMPEND(3);
}
extern "C" __global__ __launch_bounds__(512) void k_phase4(const char* __restrict__ x, char* __restrict__ ws, char* __restrict__ out) {
  typedef CfgC C;
  const int b = blockIdx.x, xcd = b & 7, idx = b >> 3;
  const int rt = (xcd >> 1) * 8 + (idx & 7), ct = (xcd & 1) * 4 + (idx >> 3);
  const int wid = __builtin_amdgcn_readfirstlane(threadIdx.x >> 6), lane = threadIdx.x & 63;
  LAS char* lds = (LAS char*)smem;
  if (ARGF) asm volatile("" ::"s"(x), "s"(ws), "s"(out));
  STAMP(3, 0);
  if (__builtin_expect(wid < 4, !LDF)) {
  const int wm = wid >> 1, wn = wid & 1;
  if (wn == 0) p4_consumer<3>(out, ws, lds, rt, ct, wm, lane);
  else p4_consumer<2>(out, ws, lds, rt, ct, wm, lane);
  return;
  }
  {
    const int w = wid - 4;
    unsigned tv[C::PT], wv[C::PW];
    make_tokv<C>(tv, w, lane, 48); make_wv<C>(wv, w, lane, 40);
    const char* xb = x + (size_t)rt * 64 * 1280 + ct * 160;
    auto ext = [&](unsigned dst) {
#pragma unroll
      for (int i = 0; i < 3; i++) {
        const unsigned ch = min((i * 4 + w) * 64 + lane, 639);
        dma16(xb, (ch / 10) * 1280 + (ch % 10) * 16, dst + (i * 4 + w) * 1024);
      }
    };
    auto fin = [&]() { waitvm<0>(); BAR(); };
    const unsigned junk = (unsigned)(size_t)lds + LDS_AUX + 7168;
    auto pre = [&]() {
      if (L2T) {
        const unsigned t = w * 64 + lane;
#pragma unroll
        for (int i = 0; i < 2; i++) {
          const unsigned la = (idx >> 3) * 320 + min(t + i * 256, 319u);
          dma4(ws + OFF_H3 + (size_t)rt * 4 * 48 * 1024, (la / 320) * 49152 + 8192 + (la % 320) * 128, junk);
        }
        const unsigned lb = (idx & 7) * 200 + min(t, 199u);
        dma4(ws + OFF_W4 + ct * 5 * 1024, (8 + lb / 40) * 40960 + (lb % 40) * 128, junk);
      }
      touch_code(idx * 4 + w, lane, junk);
    };
    loader<C, XC + 3 * L2T, 3>(w, (unsigned)(size_t)lds, ws + OFF_H3 + (size_t)rt * 4 * 48 * 1024, 4096, tv, ws + OFF_W4 + ct * 5 * 1024, 4 * 40 * 1024, wv, 12, pre, Nop(), ext, fin);
    STAMP(3, 1);
  }
}

extern "C" void kernel_launch(void* const* d_in, const int* in_sizes, int n_in, void* d_out, int out_size, void* d_ws, size_t ws_size, hipStream_t stream) {
  const char* x = (const char*)d_in[0]; const char* wqkv = (const char*)d_in[1]; const char* wo = (const char*)d_in[2];
  const char* w1 = (const char*)d_in[4]; const char* w2 = (const char*)d_in[5]; const char* w3 = (const char*)d_in[6]; const char* w4 = (const char*)d_in[7];
#define WSOFF 0
  char* ws = (char*)d_ws;
  if (WSOFF == 1) ws += (ws_size - (32u << 20)) & ~(size_t)0x1fffff;
  if (WSOFF == 2) ws += (ws_size / 2) & ~(size_t)0x1fffff;
  (void)hipFuncSetAttribute((const void*)k_phase1, hipFuncAttributeMaxDynamicSharedMemorySize, LDS_TOTAL);
  (void)hipFuncSetAttribute((const void*)k_phase2, hipFuncAttributeMaxDynamicSharedMemorySize, LDS_TOTAL);
  (void)hipFuncSetAttribute((const void*)k_phase3, hipFuncAttributeMaxDynamicSharedMemorySize, LDS_TOTAL);
  (void)hipFuncSetAttribute((const void*)k_phase4, hipFuncAttributeMaxDynamicSharedMemorySize, LDS_TOTAL);
  k_phase1<<<256, 512, LDS_TOTAL, stream>>>(x, wqkv, w1, ws, wo, w2, w3);
  k_phase2<<<256, 512, LDS_TOTAL, stream>>>(x, wqkv, wo, w2, w4, ws);
  k_phase3<<<256, 512, LDS_TOTAL, stream>>>(w3, ws);
  k_phase4<<<256, 512, LDS_TOTAL, stream>>>(x, ws, (char*)d_out);
}
```
